```python
import torch

def _softmax(x, axis=-1):
    return torch.softmax(x, dim=axis)
def _gelu(x):
    return torch.nn.functional.gelu(x, approximate="tanh")
def _silu(x):
    return torch.nn.functional.silu(x)
def _sigmoid(x):
    return torch.sigmoid(x)
def _layernorm(x, axis=-1, eps=1e-5):
    m = x.mean(dim=axis, keepdim=True)
    v = x.var(dim=axis, keepdim=True, unbiased=False)
    return (x - m) / torch.sqrt(v + eps)
def _scatter_add(tgt, idx, src, axis=0):
    return tgt.clone().index_add_(axis, idx, src)
def _segment_sum(src, seg, n):
    out = torch.zeros((n,) + src.shape[1:], dtype=src.dtype)
    return out.index_add_(0, seg, src)
def _pad(x, pw):
    flat = tuple(v for pair in reversed(pw) for v in pair)
    return torch.nn.functional.pad(x, flat)

kernel_name = 'parallel_sum__gqa_block_cached__glu_gated_bilinear__decode'

def setup_inputs():

    _w = (lambda a: (lambda t: t.to(torch.bfloat16) if t.is_floating_point() else t)(torch.as_tensor(a)))
    def _A_setup():
        B, Sq, D = 8, 1, 512
        Hq, Hkv, Sk, Dh = 32, 8, 8192, 128
        assert Hq % Hkv == 0, (Hq, Hkv)
        return {
            "x": _w((lambda *s: torch.randn(*s, dtype=torch.bfloat16) * 0.05)(B, Sq, D)),
            "k_cache": _w((lambda *s: torch.randn(*s, dtype=torch.bfloat16) * 1.6)(B, Hkv, Sk, Dh)),
            "v_cache": _w((lambda *s: torch.randn(*s, dtype=torch.bfloat16) * 0.8)(B, Hkv, Sk, Dh)),
            "wq": _w((lambda *s: torch.randn(*s, dtype=torch.bfloat16) * 1.6)(D, Hq * Dh) * (D ** -0.5)),
            "wo": _w((lambda *s: torch.randn(*s, dtype=torch.bfloat16) * 0.8)(Hq * Dh, D) * ((Hq * Dh) ** -0.5)),
        }
    _A_in = _A_setup()

    def _B_setup():
        B, S, D, F = 8, 1, 512, 1792
        return {
            "x": _w((lambda *s: torch.randn(*s, dtype=torch.bfloat16) * 0.05)(B, S, D)),
            "w_gate": _w((lambda *s: torch.randn(*s, dtype=torch.bfloat16) * 0.4)(D, F) * (D ** -0.5)),
            "w_up": _w((lambda *s: torch.randn(*s, dtype=torch.bfloat16) * 0.4)(D, F) * (D ** -0.5)),
            "w_down": _w((lambda *s: torch.randn(*s, dtype=torch.bfloat16) * 0.4)(F, D) * (F ** -0.5)),
        }
    _B_in = _B_setup()

    return {
        "A_x": _A_in["x"],
        "A_k_cache": _A_in["k_cache"],
        "A_v_cache": _A_in["v_cache"],
        "A_wq": _A_in["wq"],
        "A_wo": _A_in["wo"],
        "B_x": _B_in["x"],
        "B_w_gate": _B_in["w_gate"],
        "B_w_up": _B_in["w_up"],
        "B_w_down": _B_in["w_down"],
    }

def reference(A_x, A_k_cache, A_v_cache, A_wq, A_wo, B_x, B_w_gate, B_w_up, B_w_down):
    def _A_ref(x, k_cache, v_cache, wq, wo):
        B, Sq, D = x.shape
        Hq, Dh = 32, 128
        Hkv = k_cache.shape[1]
        rep = Hq // Hkv
        q = (lambda x, *d: x.permute(*d))((x @ wq).reshape(B, Sq, Hq, Dh), 0, 2, 1, 3)
        k = (lambda x, n, axis: torch.repeat_interleave(x, n, dim=axis))(k_cache, rep, axis=1)
        v = (lambda x, n, axis: torch.repeat_interleave(x, n, dim=axis))(v_cache, rep, axis=1)
        scores = torch.einsum("bhqd,bhkd->bhqk", q, k) * (Dh ** -0.5)
        p = _softmax(scores, axis=-1)
        o = torch.einsum("bhqk,bhkd->bhqd", p, v)
        return (lambda x, *d: x.permute(*d))(o, 0, 2, 1, 3).reshape(B, Sq, Hq * Dh) @ wo

    def _B_ref(x, w_gate, w_up, w_down):
        g = torch.einsum("bsd,df->bsf", x, w_gate)
        u = torch.einsum("bsd,df->bsf", x, w_up)
        h = g * u
        return torch.einsum("bsf,fd->bsd", h, w_down)

    x = A_x
    xn = _layernorm(x, axis=-1)
    return x + _A_ref(xn, A_k_cache, A_v_cache, A_wq, A_wo) + _B_ref(xn, B_w_gate, B_w_up, B_w_down)
```

```cpp
#include <hip/hip_runtime.h>
#include <hip/hip_bf16.h>
#include <stdint.h>
#pragma clang diagnostic ignored "-Wint-to-pointer-cast"

#define GAS __attribute__((address_space(1)))
#define LAS __attribute__((address_space(3)))
typedef unsigned u32;
typedef unsigned long long u64;
typedef u32 u32x2 __attribute__((ext_vector_type(2)));
typedef u32 u32x4 __attribute__((ext_vector_type(4)));
typedef float f32x2 __attribute__((ext_vector_type(2)));
typedef float f32x4 __attribute__((ext_vector_type(4)));
typedef __bf16 bf16x2 __attribute__((ext_vector_type(2)));
typedef __bf16 bf16x8 __attribute__((ext_vector_type(8)));
typedef short s16x4 __attribute__((ext_vector_type(4)));

#define KV_NT 1
#define PACE 8

namespace {

constexpr int DM = 512, SK = 8192, DH = 128, FF = 1792;
constexpr int NWG = 256, NTHR = 512;
constexpr int KEYS_PER_WAVE = SK / 16;
constexpr int NSTEP = KEYS_PER_WAVE / 32;
constexpr u32 SPIN_CAP = 1u << 18;

constexpr u32 WS_EPOCH = 0;
constexpr u32 WS_GQ = 4096;
constexpr u32 G1_SLOT = 1152;
constexpr u32 WS_G1 = WS_GQ + 8 * 8 * 512 * 8;
constexpr u32 WS_G2 = WS_G1 + 8 * 8 * 4 * 4 * G1_SLOT;
constexpr u32 WS_END = WS_G2 + 8 * 32 * 32 * 128;
constexpr u32 WS_STAMP = 4u << 20;

constexpr u32 L_RING = 0;
constexpr u32 L_WQ = 131072;
constexpr u32 L_MRG = L_WQ;
constexpr u32 L_MRG_ML = L_MRG + 16 * 528;
constexpr u32 L_HB = L_MRG_ML + 128;
constexpr u32 L_XN = L_WQ + 16384;
constexpr u32 XN_STRIDE = 1056;
constexpr u32 L_Q = L_XN + 8 * XN_STRIDE;
constexpr u32 Q_STRIDE = 272;
constexpr u32 L_OP = L_Q + 4 * Q_STRIDE;
constexpr u32 L_CNT = L_OP + 1024;
constexpr u32 L_END = L_CNT + 64;
static_assert(L_HB + 2048 <= L_XN, "merge + FFN exchange fit the wq stage");
static_assert(L_END <= 163840, "LDS");

constexpr float LOG2E = 1.4426950408889634f;
constexpr float QK_SCALE = 0.08838834764831845f;

__device__ __forceinline__ float bf_lo(u32 w) { return __builtin_bit_cast(float, w << 16); }
__device__ __forceinline__ float bf_hi(u32 w) { return __builtin_bit_cast(float, w & 0xffff0000u); }
__device__ __forceinline__ u32 pack_bf(float a, float b) {
  f32x2 v = {a, b};
  return __builtin_bit_cast(u32, __builtin_convertvector(v, bf16x2));
}
__device__ __forceinline__ float bfr(float a) { return bf_lo(pack_bf(a, a)); }
__device__ __forceinline__ float dot2(u32 a, u32 b, float c) {
  return __builtin_amdgcn_fdot2_f32_bf16(__builtin_bit_cast(bf16x2, a), __builtin_bit_cast(bf16x2, b), c, false);
}
template <int CTRL> __device__ __forceinline__ float dpp(float x) {
  return __builtin_bit_cast(float, __builtin_amdgcn_mov_dpp(__builtin_bit_cast(int, x), CTRL, 0xf, 0xf, true));
}
constexpr int XOR1 = 0xB1, XOR2 = 0x4E, HMIRROR = 0x141, MIRROR = 0x140, ROR8 = 0x128;
__device__ __forceinline__ float f_of(u32 x) { return __builtin_bit_cast(float, x); }
__device__ __forceinline__ u32 u_of(float x) { return __builtin_bit_cast(u32, x); }
__device__ __forceinline__ float swap16_add(float a, float b) {
  auto s = __builtin_amdgcn_permlane16_swap(u_of(a), u_of(b), false, false);
  return f_of(s[0]) + f_of(s[1]);
}
__device__ __forceinline__ float swap32_add(float a, float b) {
  auto s = __builtin_amdgcn_permlane32_swap(u_of(a), u_of(b), false, false);
  return f_of(s[0]) + f_of(s[1]);
}
__device__ __forceinline__ float rows_max(float x) {
  auto s = __builtin_amdgcn_permlane16_swap(u_of(x), u_of(x), false, false);
  x = fmaxf(f_of(s[0]), f_of(s[1]));
  auto t = __builtin_amdgcn_permlane32_swap(u_of(x), u_of(x), false, false);
  return fmaxf(f_of(t[0]), f_of(t[1]));
}
__device__ __forceinline__ float rows_sum(float x) { x = swap16_add(x, x); return swap32_add(x, x); }
__device__ __forceinline__ float wave_sum(float x) {
  x += dpp<XOR1>(x); x += dpp<XOR2>(x); x += dpp<HMIRROR>(x); x += dpp<MIRROR>(x);
  return rows_sum(x);
}

__device__ __forceinline__ void dma4(u32 ldsa, const void* sb, u32 v0, u32 v1, u32 v2, u32 v3) {
  u32 keep;
  asm volatile("s_mov_b32 %0, m0\n\ts_mov_b32 m0, %1\n\ts_nop 0\n\t"
               "global_load_lds_dwordx4 %3, %2 nt\n\t"
               "global_load_lds_dwordx4 %4, %2 offset:1024 nt\n\t"
               "global_load_lds_dwordx4 %5, %2 offset:2048 nt\n\t"
               "global_load_lds_dwordx4 %6, %2 offset:3072 nt\n\t"
               "s_mov_b32 m0, %0"
               : "=&s"(keep) : "s"(ldsa), "s"(sb), "v"(v0), "v"(v1), "v"(v2), "v"(v3) : "memory");
}
__device__ __forceinline__ void dma1(u32 ldsa, const void* sb, u32 v0) {
  u32 keep;
  asm volatile("s_mov_b32 %0, m0\n\ts_mov_b32 m0, %1\n\ts_nop 0\n\t"
               "global_load_lds_dwordx4 %3, %2\n\t"
               "s_mov_b32 m0, %0"
               : "=&s"(keep) : "s"(ldsa), "s"(sb), "v"(v0) : "memory");
}
#define WAIT_VM(n) asm volatile("s_waitcnt vmcnt(" #n ")" ::: "memory")
#define WAIT_LGKM0() asm volatile("s_waitcnt lgkmcnt(0)" ::: "memory")
#define BARRIER() do { asm volatile("s_waitcnt lgkmcnt(0)" ::: "memory"); __builtin_amdgcn_s_barrier(); asm volatile("" ::: "memory"); } while (0)

template <int N> __device__ __forceinline__ void wait_vm() {
  if constexpr (N == 0) WAIT_VM(0);
  else if constexpr (N == 4) WAIT_VM(4);
  else if constexpr (N == 8) WAIT_VM(8);
  else if constexpr (N == 12) WAIT_VM(12);
  else if constexpr (N == 16) WAIT_VM(16);
  else if constexpr (N == 24) WAIT_VM(24);
  else if constexpr (N == 28) WAIT_VM(28);
  else static_assert(N == 0, "wait");
}

__device__ __forceinline__ u32x4 lds_rd128(u32 a) { return *(const LAS u32x4*)a; }
__device__ __forceinline__ u32x2 lds_tr(u32 a) {
  return __builtin_bit_cast(u32x2, __builtin_amdgcn_ds_read_tr16_b64_v4i16((LAS s16x4*)a));
}
__device__ __forceinline__ f32x4 mfma(u32x4 a, u32x4 b, f32x4 c) {
  return __builtin_amdgcn_mfma_f32_16x16x32_bf16(__builtin_bit_cast(bf16x8, a), __builtin_bit_cast(bf16x8, b), c, 0, 0, 0);
}

#define STAMP(i) do { } while (0)

struct StreamState {
  f32x4 acc[8];
  float m, l;
};

template <int SLOT, int WK, int WV, bool ISSUE>
__device__ __forceinline__ void stream_step(StreamState& st, const u32x4 (&qf)[4], const u32 (&ka)[4], const u32 (&va)[8],
                                            u32 ring, const char* kn, const char* vn, const u32 (&vk)[4], const u32 (&vv)[2]) {
  wait_vm<WK>();
  u32x4 kf[2][4];
#pragma unroll
  for (int tt = 0; tt < 2; ++tt)
#pragma unroll
    for (int i = 0; i < 4; ++i) kf[tt][i] = lds_rd128(ka[i] + SLOT * 16384 + tt * 4096);
  WAIT_LGKM0();
  if constexpr (ISSUE) {
    dma4(ring + SLOT * 16384, kn, vk[0], vk[1], vk[2], vk[3]);
    dma4(ring + SLOT * 16384 + 4096, kn + 4096, vk[0], vk[1], vk[2], vk[3]);
  }
  f32x4 s0 = {0.f, 0.f, 0.f, 0.f}, s1 = {0.f, 0.f, 0.f, 0.f};
#pragma unroll
  for (int i = 0; i < 4; ++i) { s0 = mfma(kf[0][i], qf[i], s0); s1 = mfma(kf[1][i], qf[i], s1); }
  float t[8];
  {
    const u32 a = pack_bf(s0[0], s0[1]), b = pack_bf(s0[2], s0[3]), c = pack_bf(s1[0], s1[1]), d = pack_bf(s1[2], s1[3]);
    const u32 a2 = pack_bf(bf_lo(a) * QK_SCALE, bf_hi(a) * QK_SCALE), b2 = pack_bf(bf_lo(b) * QK_SCALE, bf_hi(b) * QK_SCALE);
    const u32 c2 = pack_bf(bf_lo(c) * QK_SCALE, bf_hi(c) * QK_SCALE), d2 = pack_bf(bf_lo(d) * QK_SCALE, bf_hi(d) * QK_SCALE);
    t[0] = bf_lo(a2); t[1] = bf_hi(a2); t[2] = bf_lo(b2); t[3] = bf_hi(b2);
    t[4] = bf_lo(c2); t[5] = bf_hi(c2); t[6] = bf_lo(d2); t[7] = bf_hi(d2);
  }
  float mx = fmaxf(fmaxf(fmaxf(t[0], t[1]), fmaxf(t[2], t[3])), fmaxf(fmaxf(t[4], t[5]), fmaxf(t[6], t[7])));
  mx = rows_max(mx);
  if (__builtin_amdgcn_ballot_w64(mx > st.m) != 0ull) {
    const float mn = fmaxf(st.m, mx);
    const float alpha = __builtin_amdgcn_exp2f((st.m - mn) * LOG2E);
    st.m = mn; st.l *= alpha;
#pragma unroll
    for (int c = 0; c < 8; ++c) st.acc[c] *= alpha;
  }
  const float mb = st.m * LOG2E;
  float p[8];
#pragma unroll
  for (int j = 0; j < 8; ++j) p[j] = __builtin_amdgcn_exp2f(fmaf(t[j], LOG2E, -mb));
  st.l += ((p[0] + p[1]) + (p[2] + p[3])) + ((p[4] + p[5]) + (p[6] + p[7]));
  const u32x4 pf = {pack_bf(p[0], p[1]), pack_bf(p[2], p[3]), pack_bf(p[4], p[5]), pack_bf(p[6], p[7])};
  wait_vm<WV>();
  u32x4 vf[8];
#pragma unroll
  for (int c = 0; c < 8; ++c) {
    const u32x2 lo = lds_tr(va[c] + SLOT * 16384 + 8192), hi = lds_tr(va[c] + SLOT * 16384 + 8192 + 4096);
    vf[c] = u32x4{lo[0], lo[1], hi[0], hi[1]};
  }
  WAIT_LGKM0();
  if constexpr (ISSUE) {
    dma4(ring + SLOT * 16384 + 8192, vn, vv[0], vv[1], vv[0], vv[1]);
    dma4(ring + SLOT * 16384 + 8192 + 4096, vn + 4096, vv[0], vv[1], vv[0], vv[1]);
  }
#pragma unroll
  for (int c = 0; c < 8; ++c) st.acc[c] = mfma(vf[c], pf, st.acc[c]);
}

}

__global__ void __launch_bounds__(NTHR, 2)
fused_decode_kernel(const u32* __restrict__ x_, const char* __restrict__ kc, const char* __restrict__ vc, const char* __restrict__ wq,
                    const char* __restrict__ wo, const char* __restrict__ wg, const char* __restrict__ wu, const char* __restrict__ wd,
                    u32* __restrict__ out, char* __restrict__ ws) {
  __shared__ __attribute__((aligned(1024))) char smem[L_END];
  const u32 lds = (u32)(size_t)(LAS char*)smem;
  const int tid = threadIdx.x, lane = tid & 63;
  const int wave = __builtin_amdgcn_readfirstlane(tid >> 6);
  const int wg_id = blockIdx.x;
  const int kvh = wg_id & 7, rr = wg_id >> 3, b = rr >> 2, sp = rr & 3;
  const int hh = kvh * 4 + sp;
  STAMP(0);

  if (wave < 4) {
    const int g = lane >> 4, r = lane & 15;
    const size_t seq = ((size_t)(b * 8 + kvh) * SK + (size_t)(sp * 4 + wave) * KEYS_PER_WAVE) * (DH * 2);
    const char* kb = kc + seq;
    const char* vb = vc + seq;
    const u32 ring = lds + L_RING + wave * 32768;
    u32 vk[4], vv[2];
#pragma unroll
    for (int n = 0; n < 4; ++n) vk[n] = g * 256 + (((r ^ g) ^ (4 * n)) << 4);
#pragma unroll
    for (int n = 0; n < 2; ++n) vv[n] = g * 256 + ((r ^ ((4 * n + g) << 1)) << 4);
    dma4(ring, kb, vk[0], vk[1], vk[2], vk[3]);
    dma4(ring + 4096, kb + 4096, vk[0], vk[1], vk[2], vk[3]);
    if constexpr (PACE >= 16) {
      dma4(ring + 8192, vb, vv[0], vv[1], vv[0], vv[1]);
      dma4(ring + 8192 + 4096, vb + 4096, vv[0], vv[1], vv[0], vv[1]);
    }
    if constexpr (PACE >= 32) {
      dma4(ring + 16384, kb + 8192, vk[0], vk[1], vk[2], vk[3]);
      dma4(ring + 16384 + 4096, kb + 8192 + 4096, vk[0], vk[1], vk[2], vk[3]);
      dma4(ring + 16384 + 8192, vb + 8192, vv[0], vv[1], vv[0], vv[1]);
      dma4(ring + 16384 + 8192 + 4096, vb + 8192 + 4096, vv[0], vv[1], vv[0], vv[1]);
    }
    BARRIER();
    if constexpr (PACE == 8) {
      WAIT_VM(4); dma4(ring + 8192, vb, vv[0], vv[1], vv[0], vv[1]);
      WAIT_VM(4); dma4(ring + 8192 + 4096, vb + 4096, vv[0], vv[1], vv[0], vv[1]);
      WAIT_VM(4); dma4(ring + 16384, kb + 8192, vk[0], vk[1], vk[2], vk[3]);
      WAIT_VM(4); dma4(ring + 16384 + 4096, kb + 8192 + 4096, vk[0], vk[1], vk[2], vk[3]);
      WAIT_VM(4); dma4(ring + 16384 + 8192, vb + 8192, vv[0], vv[1], vv[0], vv[1]);
      WAIT_VM(4); dma4(ring + 16384 + 8192 + 4096, vb + 8192 + 4096, vv[0], vv[1], vv[0], vv[1]);
    } else if constexpr (PACE == 16) {
      WAIT_VM(12); dma4(ring + 16384, kb + 8192, vk[0], vk[1], vk[2], vk[3]);
      WAIT_VM(12); dma4(ring + 16384 + 4096, kb + 8192 + 4096, vk[0], vk[1], vk[2], vk[3]);
      WAIT_VM(12); dma4(ring + 16384 + 8192, vb + 8192, vv[0], vv[1], vv[0], vv[1]);
      WAIT_VM(12); dma4(ring + 16384 + 8192 + 4096, vb + 8192 + 4096, vv[0], vv[1], vv[0], vv[1]);
    }
    STAMP(1);
    BARRIER();
    STAMP(2);
    u32x4 qf[4];
#pragma unroll
    for (int i = 0; i < 4; ++i) {
      const u32x4 v = lds_rd128(lds + L_Q + (r & 3) * Q_STRIDE + 64 * i + 16 * g);
      qf[i] = r < 4 ? v : u32x4{0u, 0u, 0u, 0u};
    }
    u32 ka[4], va[8];
#pragma unroll
    for (int i = 0; i < 4; ++i) ka[i] = ring + r * 256 + ((((4 * i + g) ^ r)) << 4);
    {
      const int q4 = r >> 2, p4 = r & 3, rho = (4 * g + q4) & 7;
      const u32 base = (4 * g + q4) * 256 + 16 * (p4 >> 1) + 8 * (p4 & 1);
#pragma unroll
      for (int c = 0; c < 8; ++c) va[c] = ring + base + ((c ^ rho) << 5);
    }
    StreamState st;
#pragma unroll
    for (int c = 0; c < 8; ++c) st.acc[c] = f32x4{0.f, 0.f, 0.f, 0.f};
    st.m = -INFINITY; st.l = 0.f;
    const char* kn = kb + 2 * 8192;
    const char* vn = vb + 2 * 8192;
    for (int t = 0; t < NSTEP - 2; t += 2) {
      stream_step<0, 24, 24, true>(st, qf, ka, va, ring, kn, vn, vk, vv);
      stream_step<1, 24, 24, true>(st, qf, ka, va, ring, kn + 8192, vn + 8192, vk, vv);
      kn += 16384; vn += 16384;
    }
    stream_step<0, 24, 16, false>(st, qf, ka, va, ring, kn, vn, vk, vv);
    stream_step<1, 8, 0, false>(st, qf, ka, va, ring, kn, vn, vk, vv);
    STAMP(3);
    const float lsum = rows_sum(st.l);
    if (r < 4) {
#pragma unroll
      for (int c = 0; c < 8; ++c) *(LAS f32x4*)(lds + L_MRG + (wave * 4 + r) * 528 + (16 * c + 4 * g) * 4) = st.acc[c];
      if (g == 0) *(LAS f32x2*)(lds + L_MRG_ML + (wave * 4 + r) * 8) = f32x2{st.m, lsum};
    }
    BARRIER();
    STAMP(4);
    return;
  }

  const int xw = wave - 4;
  const __amdgpu_buffer_rsrc_t wsr = __builtin_amdgcn_make_buffer_rsrc(ws, 0, WS_END, 0x00020000);
  const u64 epoch = __hip_atomic_load((GAS u64*)(ws + WS_EPOCH), __ATOMIC_RELAXED, __HIP_MEMORY_SCOPE_AGENT);
  const u32x4 xa = ((const GAS u32x4*)x_)[(2 * xw) * 64 + lane];
  const u32x4 xb = ((const GAS u32x4*)x_)[(2 * xw + 1) * 64 + lane];
  const u32 xres = ((const GAS u32*)x_)[b * 256 + hh * 8 + (lane & 7)];
  const u32 g1_in = WS_G1 + (((b * 8 + kvh) * 4 + sp) * 4) * G1_SLOT;
  const u32 g2_in = WS_G2 + (b * 32 + hh) * 4096;
  {
    const u32x4 z = {0u, 0u, 0u, 0u};
    __builtin_amdgcn_raw_buffer_store_b128(z, wsr, g1_in + xw * G1_SLOT + lane * 16, 0, 16);
    if (lane == 0) __builtin_amdgcn_raw_buffer_store_b128(z, wsr, g1_in + xw * G1_SLOT + 1024, 0, 16);
    __builtin_amdgcn_raw_buffer_store_b128(z, wsr, g2_in + xw * 1024 + lane * 16, 0, 16);
  }
  {
    const char* wsrc = wq + (kvh * 512 + rr * 16) * 2;
    const u32 vo = (lane >> 1) * 8192 + (lane & 1) * 16;
#pragma unroll
    for (int j = 0; j < 4; ++j) dma1(lds + L_WQ + (xw * 4 + j) * 1024, wsrc + (size_t)(xw * 4 + j) * 32 * 8192, vo);
  }
  if (tid == 256) *(LAS u32*)(lds + L_CNT) = 0u;
#pragma unroll
  for (int rw = 0; rw < 2; ++rw) {
    const u32x4 xv = rw ? xb : xa;
    float v[8] = {bf_lo(xv[0]), bf_hi(xv[0]), bf_lo(xv[1]), bf_hi(xv[1]), bf_lo(xv[2]), bf_hi(xv[2]), bf_lo(xv[3]), bf_hi(xv[3])};
    const float mean = wave_sum(((v[0] + v[1]) + (v[2] + v[3])) + ((v[4] + v[5]) + (v[6] + v[7]))) * (1.0f / DM);
    float ss = 0.f;
#pragma unroll
    for (int j = 0; j < 8; ++j) { const float d = v[j] - mean; ss = fmaf(d, d, ss); }
    const float var = wave_sum(ss) * (1.0f / DM);
    const float mb = bfr(mean), den = bfr(sqrtf(bfr(bfr(var) + 1e-5f)));
    u32x4 o;
#pragma unroll
    for (int j = 0; j < 4; ++j) o[j] = pack_bf(bfr(v[2 * j] - mb) / den, bfr(v[2 * j + 1] - mb) / den);
    *(LAS u32x4*)(lds + L_XN + (2 * xw + rw) * XN_STRIDE + lane * 16) = o;
  }
  WAIT_VM(0);
  BARRIER();
  STAMP(1);
  const u64 tagq = (epoch + 1ull) & 0xFFFFFFFFFFFFull;
  if (xw == 0) {
    const int g = lane >> 4, r = lane & 15;
    const u32 aa = lds + L_XN + (r & 7) * XN_STRIDE + 16 * g;
    const u32 ba = lds + L_WQ + (8 * g + (r >> 2)) * 32 + 8 * (r & 3);
    f32x4 c0 = {0.f, 0.f, 0.f, 0.f}, c1 = {0.f, 0.f, 0.f, 0.f};
#pragma unroll
    for (int i = 0; i < 16; i += 2) {
      const u32x2 l0 = lds_tr(ba + i * 1024), h0 = lds_tr(ba + i * 1024 + 128);
      const u32x2 l1 = lds_tr(ba + i * 1024 + 1024), h1 = lds_tr(ba + i * 1024 + 1024 + 128);
      c0 = mfma(lds_rd128(aa + 64 * i), u32x4{l0[0], l0[1], h0[0], h0[1]}, c0);
      c1 = mfma(lds_rd128(aa + 64 * i + 64), u32x4{l1[0], l1[1], h1[0], h1[1]}, c1);
    }
    if (g < 2) {
#pragma unroll
      for (int j = 0; j < 4; ++j) {
        const u32 qb = pack_bf(c0[j] + c1[j], 0.f) & 0xffffu;
        __hip_atomic_store((GAS u64*)(ws + WS_GQ) + ((kvh * 8 + 4 * g + j) * 512 + rr * 16 + r), (tagq << 16) | qb,
                           __ATOMIC_RELAXED, __HIP_MEMORY_SCOPE_AGENT);
      }
    }
  }
  {
    const u32 qa = WS_GQ + ((kvh * 8 + b) * 512 + xw * 128 + 2 * lane) * 8;
    const u32 tlo = (u32)tagq & 0xffffu, thi = (u32)(tagq >> 16);
    u32x4 v;
    for (u32 spin = 0; spin < SPIN_CAP; ++spin) {
      asm volatile("" ::: "memory");
      v = __builtin_amdgcn_raw_buffer_load_b128(wsr, qa, 0, 16);
      const bool ok = (v[0] >> 16) == tlo && v[1] == thi && (v[2] >> 16) == tlo && v[3] == thi;
      if (__builtin_amdgcn_ballot_w64(!ok) == 0ull) break;
      __builtin_amdgcn_s_sleep(1);
    }
    *(LAS u32*)(lds + L_Q + xw * Q_STRIDE + lane * 4) = (v[0] & 0xffffu) | (v[2] << 16);
  }
  STAMP(2);
  BARRIER();

  const int rq = lane >> 4, cc = lane & 15;
  float acc[8];
#pragma unroll
  for (int j = 0; j < 8; ++j) acc[j] = 0.f;
  const u32 opx = lds + L_OP + xw * 256;
  if (hh < FF / 64) {
    const int r8 = lane >> 3, c8 = lane & 7;
    const size_t go = (size_t)(128 * xw + r8) * (FF * 2) + hh * 128 + c8 * 16;
    u32x4 wgv[16], wuv[16];
#pragma unroll
    for (int n = 0; n < 16; ++n) wgv[n] = *(const GAS u32x4*)(wg + go + (size_t)n * 8 * (FF * 2));
#pragma unroll
    for (int n = 0; n < 16; ++n) wuv[n] = *(const GAS u32x4*)(wu + go + (size_t)n * 8 * (FF * 2));
    float ag[8], au[8];
#pragma unroll
    for (int j = 0; j < 8; ++j) { ag[j] = 0.f; au[j] = 0.f; }
#pragma unroll
    for (int n = 0; n < 16; ++n) {
      const u32 xv = *(const LAS unsigned short*)(lds + L_XN + b * XN_STRIDE + (128 * xw + 8 * n + r8) * 2);
      const u32 xh = xv << 16;
#pragma unroll
      for (int j = 0; j < 4; ++j) {
        ag[2 * j] = dot2(wgv[n][j], xv, ag[2 * j]); ag[2 * j + 1] = dot2(wgv[n][j], xh, ag[2 * j + 1]);
        au[2 * j] = dot2(wuv[n][j], xv, au[2 * j]); au[2 * j + 1] = dot2(wuv[n][j], xh, au[2 * j + 1]);
      }
    }
    float s8[8], s4[4];
#pragma unroll
    for (int j = 0; j < 8; ++j) { ag[j] += dpp<ROR8>(ag[j]); au[j] += dpp<ROR8>(au[j]); s8[j] = swap16_add(ag[j], au[j]); }
#pragma unroll
    for (int j = 0; j < 4; ++j) s4[j] = swap32_add(s8[j], s8[j + 4]);
    if ((lane & 8) == 0)
      *(LAS f32x4*)(lds + L_HB + xw * 512 + (rq & 1) * 256 + (8 * c8 + 4 * (rq >> 1)) * 4) = f32x4{s4[0], s4[1], s4[2], s4[3]};
    u32x4 wdv[16];
#pragma unroll
    for (int n = 0; n < 16; ++n) wdv[n] = *(const GAS u32x4*)(wd + (size_t)(hh * 64 + 4 * n + rq) * 1024 + xw * 256 + cc * 16);
    WAIT_LGKM0();
    if (lane == 0) __hip_atomic_fetch_add((LAS u32*)(lds + L_CNT), 1u, __ATOMIC_RELAXED, __HIP_MEMORY_SCOPE_WORKGROUP);
    for (u32 spin = 0; spin < SPIN_CAP; ++spin) {
      if (__builtin_amdgcn_readfirstlane(*(volatile LAS u32*)(lds + L_CNT)) >= 4u) break;
      __builtin_amdgcn_s_sleep(1);
    }
    asm volatile("" ::: "memory");
    float gs = 0.f, us = 0.f;
#pragma unroll
    for (int w = 0; w < 4; ++w) {
      gs += *(const LAS float*)(lds + L_HB + w * 512 + lane * 4);
      us += *(const LAS float*)(lds + L_HB + w * 512 + 256 + lane * 4);
    }
    const u32 hb = pack_bf(bfr(gs) * bfr(us), 0.f) & 0xffffu;
    *(LAS u32*)(opx + ((lane & 3) * 16 + (lane >> 2)) * 4) = hb;
    WAIT_LGKM0();
#pragma unroll
    for (int n4 = 0; n4 < 4; ++n4) {
      const u32x4 hv = lds_rd128(opx + rq * 64 + n4 * 16);
#pragma unroll
      for (int e = 0; e < 4; ++e) {
        const u32 hl = hv[e], hh2 = hl << 16;
#pragma unroll
        for (int j = 0; j < 4; ++j) {
          acc[2 * j] = dot2(wdv[4 * n4 + e][j], hl, acc[2 * j]);
          acc[2 * j + 1] = dot2(wdv[4 * n4 + e][j], hh2, acc[2 * j + 1]);
        }
      }
    }
  }
  u32 wp[16][8];
  {
    u32x4 w0[16], w1[16];
#pragma unroll
    for (int n = 0; n < 16; ++n) {
      const char* p = wo + (size_t)(hh * 128 + 8 * n + 2 * rq) * 1024 + xw * 256 + cc * 16;
      w0[n] = *(const GAS u32x4*)p; w1[n] = *(const GAS u32x4*)(p + 1024);
    }
#pragma unroll
    for (int n = 0; n < 16; ++n)
#pragma unroll
      for (int j = 0; j < 4; ++j) {
        wp[n][2 * j] = __builtin_amdgcn_perm(w1[n][j], w0[n][j], 0x05040100u);
        wp[n][2 * j + 1] = __builtin_amdgcn_perm(w1[n][j], w0[n][j], 0x07060302u);
      }
  }
  {
    const u32 ra = WS_GQ + (((lane >> 2) & 7) * 8 * 512 + (b * 4 + (lane & 3)) * 16) * 8;
    const u32 tlo = (u32)tagq & 0xffffu, thi = (u32)(tagq >> 16);
    for (u32 spin = 0; spin < SPIN_CAP; ++spin) {
      asm volatile("" ::: "memory");
      const u32x2 v = __builtin_amdgcn_raw_buffer_load_b64(wsr, ra, 0, 16);
      const bool ok = (v[0] >> 16) == tlo && v[1] == thi;
      if (__builtin_amdgcn_ballot_w64(!ok) == 0ull) break;
      __builtin_amdgcn_s_sleep(1);
    }
  }
  STAMP(3);
  BARRIER();
  STAMP(4);
  {
    float mw[4], lw[4]; f32x2 ow[4];
#pragma unroll
    for (int w = 0; w < 4; ++w) {
      const f32x2 ml = *(const LAS f32x2*)(lds + L_MRG_ML + (w * 4 + xw) * 8);
      mw[w] = ml[0]; lw[w] = ml[1];
      ow[w] = *(const LAS f32x2*)(lds + L_MRG + (w * 4 + xw) * 528 + lane * 8);
    }
    const float M = fmaxf(fmaxf(mw[0], mw[1]), fmaxf(mw[2], mw[3]));
    float L = 0.f, o0 = 0.f, o1 = 0.f;
#pragma unroll
    for (int w = 0; w < 4; ++w) {
      const float e = __builtin_amdgcn_exp2f((mw[w] - M) * LOG2E);
      L = fmaf(e, lw[w], L); o0 = fmaf(e, ow[w][0], o0); o1 = fmaf(e, ow[w][1], o1);
    }
    const u32 dst = WS_G1 + ((((b * 8 + kvh) * 4 + xw) * 4) + sp) * G1_SLOT;
    __builtin_amdgcn_raw_buffer_store_b128(u32x4{u_of(o0), 1u, u_of(o1), 1u}, wsr, dst + lane * 16, 0, 16);
    if (lane == 0) __builtin_amdgcn_raw_buffer_store_b128(u32x4{u_of(M), 1u, u_of(L), 1u}, wsr, dst + 1024, 0, 16);
  }
  {
    u32x4 d[4], ml[4];
    for (u32 spin = 0; spin < SPIN_CAP; ++spin) {
      asm volatile("" ::: "memory");
      bool ok = true;
#pragma unroll
      for (int s = 0; s < 4; ++s) {
        d[s] = __builtin_amdgcn_raw_buffer_load_b128(wsr, g1_in + s * G1_SLOT + lane * 16, 0, 16);
        ml[s] = __builtin_amdgcn_raw_buffer_load_b128(wsr, g1_in + s * G1_SLOT + 1024, 0, 16);
      }
#pragma unroll
      for (int s = 0; s < 4; ++s) ok = ok && d[s][1] == 1u && d[s][3] == 1u && ml[s][1] == 1u && ml[s][3] == 1u;
      if (__builtin_amdgcn_ballot_w64(!ok) == 0ull) break;
    }
    STAMP(5);
    const float M = fmaxf(fmaxf(f_of(ml[0][0]), f_of(ml[1][0])), fmaxf(f_of(ml[2][0]), f_of(ml[3][0])));
    float L = 0.f, o0 = 0.f, o1 = 0.f;
#pragma unroll
    for (int s = 0; s < 4; ++s) {
      const float e = __builtin_amdgcn_exp2f((f_of(ml[s][0]) - M) * LOG2E);
      L = fmaf(e, f_of(ml[s][2]), L); o0 = fmaf(e, f_of(d[s][0]), o0); o1 = fmaf(e, f_of(d[s][2]), o1);
    }
    const float inv = 1.0f / L;
    *(LAS u32*)(opx + ((lane & 3) * 16 + (lane >> 2)) * 4) = pack_bf(o0 * inv, o1 * inv);
    WAIT_LGKM0();
#pragma unroll
    for (int n4 = 0; n4 < 4; ++n4) {
      const u32x4 ov = lds_rd128(opx + rq * 64 + n4 * 16);
#pragma unroll
      for (int e = 0; e < 4; ++e)
#pragma unroll
        for (int j = 0; j < 8; ++j) acc[j] = dot2(wp[4 * n4 + e][j], ov[e], acc[j]);
    }
    float s4[4];
#pragma unroll
    for (int j = 0; j < 4; ++j) s4[j] = swap16_add(acc[j], acc[j + 4]);
    const float v0 = swap32_add(s4[0], s4[2]), v1 = swap32_add(s4[1], s4[3]);
    const int col = 128 * xw + 8 * cc + ((rq & 1) * 4 + (rq >> 1) * 2);
    __builtin_amdgcn_raw_buffer_store_b128(u32x4{u_of(v0), 1u, u_of(v1), 1u}, wsr,
                                           WS_G2 + ((b * 32 + (col >> 4)) * 32 + hh) * 128 + (col & 15) * 8, 0, 16);
  }
  if (xw != 0) return;
  {
    u32x4 d[4];
    for (u32 spin = 0; spin < SPIN_CAP; ++spin) {
      asm volatile("" ::: "memory");
      bool ok = true;
#pragma unroll
      for (int m = 0; m < 4; ++m) d[m] = __builtin_amdgcn_raw_buffer_load_b128(wsr, g2_in + m * 1024 + lane * 16, 0, 16);
#pragma unroll
      for (int m = 0; m < 4; ++m) ok = ok && d[m][1] == 1u && d[m][3] == 1u;
      if (__builtin_amdgcn_ballot_w64(!ok) == 0ull) break;
    }
    STAMP(6);
    float v0 = (f_of(d[0][0]) + f_of(d[1][0])) + (f_of(d[2][0]) + f_of(d[3][0]));
    float v1 = (f_of(d[0][2]) + f_of(d[1][2])) + (f_of(d[2][2]) + f_of(d[3][2]));
    v0 += dpp<ROR8>(v0); v1 += dpp<ROR8>(v1);
    v0 = rows_sum(v0); v1 = rows_sum(v1);
    if (lane < 8) {
      *(GAS u32*)(out + b * 256 + hh * 8 + lane) = pack_bf(bf_lo(xres) + v0, bf_hi(xres) + v1);
    }
    if (wg_id == 0 && lane == 0)
      __hip_atomic_store((GAS u64*)(ws + WS_EPOCH), epoch + 1ull, __ATOMIC_RELAXED, __HIP_MEMORY_SCOPE_AGENT);
    STAMP(7);
  }
}

extern "C" void kernel_launch(void* const* d_in, const int* in_sizes, int n_in, void* d_out, int out_size, void* d_ws,
                              size_t ws_size, hipStream_t stream) {
  (void)in_sizes; (void)n_in; (void)out_size; (void)ws_size;
  hipLaunchKernelGGL(fused_decode_kernel, dim3(NWG), dim3(NTHR), 0, stream, (const u32*)d_in[0], (const char*)d_in[1],
                     (const char*)d_in[2], (const char*)d_in[3], (const char*)d_in[4], (const char*)d_in[6],
                     (const char*)d_in[7], (const char*)d_in[8], (u32*)d_out, (char*)d_ws);
}
```

```cpp
#include <hip/hip_runtime.h>
#include <hip/hip_bf16.h>
#include <stdint.h>
#pragma clang diagnostic ignored "-Wint-to-pointer-cast"

#define GAS __attribute__((address_space(1)))
#define LAS __attribute__((address_space(3)))
typedef unsigned u32;
typedef unsigned long long u64;
typedef u32 u32x2 __attribute__((ext_vector_type(2)));
typedef u32 u32x4 __attribute__((ext_vector_type(4)));
typedef float f32x2 __attribute__((ext_vector_type(2)));
typedef float f32x4 __attribute__((ext_vector_type(4)));
typedef __bf16 bf16x2 __attribute__((ext_vector_type(2)));
typedef __bf16 bf16x8 __attribute__((ext_vector_type(8)));
typedef short s16x4 __attribute__((ext_vector_type(4)));

#define KV_NT 1
#define WIN 12
#define ROT 1

namespace {

constexpr int DM = 512, SK = 8192, DH = 128, FF = 1792;
constexpr int NWG = 256, NTHR = 512;
constexpr int KEYS_PER_WAVE = SK / 16;
constexpr int NSTEP = KEYS_PER_WAVE / 32;
constexpr u32 SPIN_CAP = 1u << 18;

constexpr u32 WS_EPOCH = 0;
constexpr u32 WS_RDY = 4096;
constexpr u32 WS_GQ = 8192;
constexpr u32 WS_GH = WS_GQ + 8 * 8 * 512 * 8;
constexpr u32 G1_SLOT = 1152;
constexpr u32 WS_G1 = WS_GH + 8 * FF * 8;
constexpr u32 WS_G2 = WS_G1 + 8 * 4 * 8 * 4 * G1_SLOT;
constexpr u32 WS_END = WS_G2 + 8 * 32 * 32 * 128;
constexpr u32 WS_STAMP = 4u << 20;

constexpr u32 L_RING = 0;
constexpr u32 L_STG = 131072;
constexpr u32 L_XN = L_STG + 16384;
constexpr u32 XN_STRIDE = 1056;
constexpr u32 L_Q = L_XN + 8 * XN_STRIDE;
constexpr u32 Q_STRIDE = 272;
constexpr u32 L_RED = L_Q + 4 * Q_STRIDE;
constexpr u32 L_O = L_RED;
constexpr u32 L_H = L_RED + 4096;
constexpr u32 H_STRIDE = 144;
constexpr u32 L_CNT = L_H + 8 * H_STRIDE;
constexpr u32 L_END = L_CNT + 64;
constexpr u32 MRG_ML = 4 * 528;
static_assert(L_END <= 163840, "LDS");

constexpr float LOG2E = 1.4426950408889634f;
constexpr float QK_SCALE = 0.08838834764831845f;

__device__ __forceinline__ float bf_lo(u32 w) { return __builtin_bit_cast(float, w << 16); }
__device__ __forceinline__ float bf_hi(u32 w) { return __builtin_bit_cast(float, w & 0xffff0000u); }
__device__ __forceinline__ u32 pack_bf(float a, float b) {
  f32x2 v = {a, b};
  return __builtin_bit_cast(u32, __builtin_convertvector(v, bf16x2));
}
__device__ __forceinline__ float bfr(float a) { return bf_lo(pack_bf(a, a)); }
template <int CTRL> __device__ __forceinline__ float dpp(float x) {
  return __builtin_bit_cast(float, __builtin_amdgcn_mov_dpp(__builtin_bit_cast(int, x), CTRL, 0xf, 0xf, true));
}
constexpr int XOR1 = 0xB1, XOR2 = 0x4E, HMIRROR = 0x141, MIRROR = 0x140, ROR8 = 0x128;
__device__ __forceinline__ float f_of(u32 x) { return __builtin_bit_cast(float, x); }
__device__ __forceinline__ u32 u_of(float x) { return __builtin_bit_cast(u32, x); }
__device__ __forceinline__ float rows_max(float x) {
  auto s = __builtin_amdgcn_permlane16_swap(u_of(x), u_of(x), false, false);
  x = fmaxf(f_of(s[0]), f_of(s[1]));
  auto t = __builtin_amdgcn_permlane32_swap(u_of(x), u_of(x), false, false);
  return fmaxf(f_of(t[0]), f_of(t[1]));
}
__device__ __forceinline__ float rows_sum(float x) {
  auto s = __builtin_amdgcn_permlane16_swap(u_of(x), u_of(x), false, false);
  x = f_of(s[0]) + f_of(s[1]);
  auto t = __builtin_amdgcn_permlane32_swap(u_of(x), u_of(x), false, false);
  return f_of(t[0]) + f_of(t[1]);
}
__device__ __forceinline__ float wave_sum(float x) {
  x += dpp<XOR1>(x); x += dpp<XOR2>(x); x += dpp<HMIRROR>(x); x += dpp<MIRROR>(x);
  return rows_sum(x);
}

__device__ __forceinline__ void dma4(u32 ldsa, const void* sb, u32 v0, u32 v1, u32 v2, u32 v3) {
  u32 keep;
  asm volatile("s_mov_b32 %0, m0\n\ts_mov_b32 m0, %1\n\ts_nop 0\n\t"
               "global_load_lds_dwordx4 %3, %2 nt\n\t"
               "global_load_lds_dwordx4 %4, %2 offset:1024 nt\n\t"
               "global_load_lds_dwordx4 %5, %2 offset:2048 nt\n\t"
               "global_load_lds_dwordx4 %6, %2 offset:3072 nt\n\t"
               "s_mov_b32 m0, %0"
               : "=&s"(keep) : "s"(ldsa), "s"(sb), "v"(v0), "v"(v1), "v"(v2), "v"(v3) : "memory");
}
__device__ __forceinline__ void dma1(u32 ldsa, const void* sb, u32 v0) {
  u32 keep;
  asm volatile("s_mov_b32 %0, m0\n\ts_mov_b32 m0, %1\n\ts_nop 0\n\t"
               "global_load_lds_dwordx4 %3, %2\n\t"
               "s_mov_b32 m0, %0"
               : "=&s"(keep) : "s"(ldsa), "s"(sb), "v"(v0) : "memory");
}
#define WAIT_LGKM0() asm volatile("s_waitcnt lgkmcnt(0)" ::: "memory")
#define BARRIER() do { asm volatile("s_waitcnt lgkmcnt(0)" ::: "memory"); __builtin_amdgcn_s_barrier(); asm volatile("" ::: "memory"); } while (0)
template <int N> __device__ __forceinline__ void wait_vm() { asm volatile("s_waitcnt vmcnt(%0)" :: "i"(N) : "memory"); }

__device__ __forceinline__ u32x4 lds_rd128(u32 a) { return *(const LAS u32x4*)a; }
__device__ __forceinline__ u32x2 lds_tr(u32 a) {
  return __builtin_bit_cast(u32x2, __builtin_amdgcn_ds_read_tr16_b64_v4i16((LAS s16x4*)a));
}
__device__ __forceinline__ f32x4 mfma(u32x4 a, u32x4 b, f32x4 c) {
  return __builtin_amdgcn_mfma_f32_16x16x32_bf16(__builtin_bit_cast(bf16x8, a), __builtin_bit_cast(bf16x8, b), c, 0, 0, 0);
}
__device__ __forceinline__ u32x4 stage_frag(u32 ba, int i) {
  const u32x2 lo = lds_tr(ba + i * 1024), hi = lds_tr(ba + i * 1024 + 128);
  return u32x4{lo[0], lo[1], hi[0], hi[1]};
}
__device__ __forceinline__ void aux_sync(u32 cnt, int lane) {
  WAIT_LGKM0();
  if (lane == 0) __hip_atomic_fetch_add((LAS u32*)cnt, 1u, __ATOMIC_RELAXED, __HIP_MEMORY_SCOPE_WORKGROUP);
#pragma unroll 1
  for (u32 spin = 0; spin < SPIN_CAP; ++spin) {
    if (__builtin_amdgcn_readfirstlane(*(volatile LAS u32*)cnt) >= 4u) break;
  }
  asm volatile("" ::: "memory");
}
__device__ __forceinline__ bool all_lanes(bool ok) { return __builtin_amdgcn_ballot_w64(!ok) == 0ull; }

#define STAMP(i) do { } while (0)

struct StreamState {
  f32x4 acc[8];
  float m, l;
};

__device__ __forceinline__ void issue_k(u32 ldsa, const char* src, const u32 (&vk)[4]) {
  wait_vm<WIN - 4>();
  dma4(ldsa, src, vk[0], vk[1], vk[2], vk[3]);
}
__device__ __forceinline__ void issue_v(u32 ldsa, const char* src, const u32 (&vv)[2]) {
  wait_vm<WIN - 4>();
  dma4(ldsa, src, vv[0], vv[1], vv[0], vv[1]);
}

template <int SLOT, int WK, int WV, bool ISSUE>
__device__ __forceinline__ void stream_step(StreamState& st, const u32x4 (&qf)[4], const u32 (&ka)[4], const u32 (&va)[8],
                                            u32 ring, const char* kn, const char* vn, const u32 (&vk)[4], const u32 (&vv)[2]) {
  wait_vm<WK>();
  u32x4 kf[2][4];
#pragma unroll
  for (int tt = 0; tt < 2; ++tt)
#pragma unroll
    for (int i = 0; i < 4; ++i) kf[tt][i] = lds_rd128(ka[i] + SLOT * 16384 + tt * 4096);
  WAIT_LGKM0();
  if constexpr (ISSUE) {
    issue_k(ring + SLOT * 16384, kn, vk);
    issue_k(ring + SLOT * 16384 + 4096, kn + 4096, vk);
  }
  f32x4 s0 = {0.f, 0.f, 0.f, 0.f}, s1 = {0.f, 0.f, 0.f, 0.f};
#pragma unroll
  for (int i = 0; i < 4; ++i) { s0 = mfma(kf[0][i], qf[i], s0); s1 = mfma(kf[1][i], qf[i], s1); }
  float t[8];
  {
    const u32 a = pack_bf(s0[0], s0[1]), b = pack_bf(s0[2], s0[3]), c = pack_bf(s1[0], s1[1]), d = pack_bf(s1[2], s1[3]);
    const u32 a2 = pack_bf(bf_lo(a) * QK_SCALE, bf_hi(a) * QK_SCALE), b2 = pack_bf(bf_lo(b) * QK_SCALE, bf_hi(b) * QK_SCALE);
    const u32 c2 = pack_bf(bf_lo(c) * QK_SCALE, bf_hi(c) * QK_SCALE), d2 = pack_bf(bf_lo(d) * QK_SCALE, bf_hi(d) * QK_SCALE);
    t[0] = bf_lo(a2); t[1] = bf_hi(a2); t[2] = bf_lo(b2); t[3] = bf_hi(b2);
    t[4] = bf_lo(c2); t[5] = bf_hi(c2); t[6] = bf_lo(d2); t[7] = bf_hi(d2);
  }
  float mx = fmaxf(fmaxf(fmaxf(t[0], t[1]), fmaxf(t[2], t[3])), fmaxf(fmaxf(t[4], t[5]), fmaxf(t[6], t[7])));
  mx = rows_max(mx);
  if (__builtin_amdgcn_ballot_w64(mx > st.m) != 0ull) {
    const float mn = fmaxf(st.m, mx);
    const float alpha = __builtin_amdgcn_exp2f((st.m - mn) * LOG2E);
    st.m = mn; st.l *= alpha;
#pragma unroll
    for (int c = 0; c < 8; ++c) st.acc[c] *= alpha;
  }
  const float mb = st.m * LOG2E;
  float p[8];
#pragma unroll
  for (int j = 0; j < 8; ++j) p[j] = __builtin_amdgcn_exp2f(fmaf(t[j], LOG2E, -mb));
  st.l += ((p[0] + p[1]) + (p[2] + p[3])) + ((p[4] + p[5]) + (p[6] + p[7]));
  const u32x4 pf = {pack_bf(p[0], p[1]), pack_bf(p[2], p[3]), pack_bf(p[4], p[5]), pack_bf(p[6], p[7])};
  wait_vm<WV>();
  u32x4 vf[8];
#pragma unroll
  for (int c = 0; c < 8; ++c) {
    const u32x2 lo = lds_tr(va[c] + SLOT * 16384 + 8192), hi = lds_tr(va[c] + SLOT * 16384 + 8192 + 4096);
    vf[c] = u32x4{lo[0], lo[1], hi[0], hi[1]};
  }
  WAIT_LGKM0();
  if constexpr (ISSUE) {
    issue_v(ring + SLOT * 16384 + 8192, vn, vv);
    issue_v(ring + SLOT * 16384 + 8192 + 4096, vn + 4096, vv);
  }
#pragma unroll
  for (int c = 0; c < 8; ++c) st.acc[c] = mfma(vf[c], pf, st.acc[c]);
}

}

__global__ void __launch_bounds__(NTHR, 2)
fused_decode_kernel(const u32* __restrict__ x_, const char* __restrict__ kc, const char* __restrict__ vc, const char* __restrict__ wq,
                    const char* __restrict__ wo, const char* __restrict__ wg, const char* __restrict__ wu, const char* __restrict__ wd,
                    u32* __restrict__ out, char* __restrict__ ws) {
  __shared__ __attribute__((aligned(1024))) char smem[L_END];
  const u32 lds = (u32)(size_t)(LAS char*)smem;
  const int tid = threadIdx.x, lane = tid & 63;
  const int wave = __builtin_amdgcn_readfirstlane(tid >> 6);
  const int wg_id = blockIdx.x;
  const int kvh = wg_id & 7, rr = wg_id >> 3, b = rr >> 2, sp = rr & 3;
  const int g = lane >> 4, r = lane & 15;
  STAMP(0);

  if (wave < 4) {
    const size_t seq = ((size_t)(b * 8 + kvh) * SK + (size_t)(sp * 4 + wave) * KEYS_PER_WAVE) * (DH * 2);
    const char* kb = kc + seq;
    const char* vb = vc + seq;
    const u32 ring = lds + L_RING + wave * 32768;
    const int rot = ROT ? ((wg_id * 37 + wave * 11) & (NSTEP - 1)) : 0;
    u32 vk[4], vv[2];
#pragma unroll
    for (int n = 0; n < 4; ++n) vk[n] = g * 256 + (((r ^ g) ^ (4 * n)) << 4);
#pragma unroll
    for (int n = 0; n < 2; ++n) vv[n] = g * 256 + ((r ^ ((4 * n + g) << 1)) << 4);
    BARRIER();
    {
      const u32 o0 = rot * 8192, o1 = ((rot + 1) & (NSTEP - 1)) * 8192;
      issue_k(ring, kb + o0, vk); issue_k(ring + 4096, kb + o0 + 4096, vk);
      issue_v(ring + 8192, vb + o0, vv); issue_v(ring + 8192 + 4096, vb + o0 + 4096, vv);
      issue_k(ring + 16384, kb + o1, vk); issue_k(ring + 16384 + 4096, kb + o1 + 4096, vk);
      issue_v(ring + 16384 + 8192, vb + o1, vv); issue_v(ring + 16384 + 8192 + 4096, vb + o1 + 4096, vv);
    }
    STAMP(1);
    BARRIER();
    STAMP(2);
    u32x4 qf[4];
#pragma unroll
    for (int i = 0; i < 4; ++i) {
      const u32x4 v = lds_rd128(lds + L_Q + (r & 3) * Q_STRIDE + 64 * i + 16 * g);
      qf[i] = r < 4 ? v : u32x4{0u, 0u, 0u, 0u};
    }
    u32 ka[4], va[8];
#pragma unroll
    for (int i = 0; i < 4; ++i) ka[i] = ring + r * 256 + ((((4 * i + g) ^ r)) << 4);
    {
      const int q4 = r >> 2, p4 = r & 3, rho = (4 * g + q4) & 7;
      const u32 base = (4 * g + q4) * 256 + 16 * (p4 >> 1) + 8 * (p4 & 1);
#pragma unroll
      for (int c = 0; c < 8; ++c) va[c] = ring + base + ((c ^ rho) << 5);
    }
    StreamState st;
#pragma unroll
    for (int c = 0; c < 8; ++c) st.acc[c] = f32x4{0.f, 0.f, 0.f, 0.f};
    st.m = -INFINITY; st.l = 0.f;
#pragma unroll 1
    for (int t = 0; t < NSTEP - 2; t += 2) {
      const u32 o0 = ((t + 2 + rot) & (NSTEP - 1)) * 8192, o1 = ((t + 3 + rot) & (NSTEP - 1)) * 8192;
      stream_step<0, 24, 24, true>(st, qf, ka, va, ring, kb + o0, vb + o0, vk, vv);
      stream_step<1, 24, 24, true>(st, qf, ka, va, ring, kb + o1, vb + o1, vk, vv);
    }
    stream_step<0, 24, 16, false>(st, qf, ka, va, ring, kb, vb, vk, vv);
    stream_step<1, 8, 0, false>(st, qf, ka, va, ring, kb, vb, vk, vv);
    STAMP(3);
    const float lsum = rows_sum(st.l);
    if (r < 4) {
#pragma unroll
      for (int c = 0; c < 8; ++c) *(LAS f32x4*)(ring + r * 528 + (16 * c + 4 * g) * 4) = st.acc[c];
      if (g == 0) *(LAS f32x2*)(ring + MRG_ML + r * 8) = f32x2{st.m, lsum};
    }
    BARRIER();
    STAMP(4);
    return;
  }

  __builtin_amdgcn_s_setprio(3);
  const int xw = wave - 4;
  const u32 stg = lds + L_STG + xw * 4096;
  const u32 vseg8k = (lane >> 1) * 8192 + (lane & 1) * 16;
  const u32x4 xa = ((const GAS u32x4*)x_)[(2 * xw) * 64 + lane];
  const u32x4 xb = ((const GAS u32x4*)x_)[(2 * xw + 1) * 64 + lane];
  {
    const char* wsrc = wq + (size_t)(128 * xw) * 8192 + (kvh * 512 + rr * 16) * 2;
#pragma unroll
    for (int j = 0; j < 4; ++j) dma1(stg + j * 1024, wsrc + (size_t)j * 32 * 8192, vseg8k);
  }
  const u64 epoch = __hip_atomic_load((GAS u64*)(ws + WS_EPOCH), __ATOMIC_RELAXED, __HIP_MEMORY_SCOPE_AGENT);
  if (tid < 256 + 16) *(LAS u32*)(lds + L_CNT + (tid - 256) * 4) = 0u;
  BARRIER();
  const __amdgpu_buffer_rsrc_t wsr = __builtin_amdgcn_make_buffer_rsrc(ws, 0, WS_END, 0x00020000);
#pragma unroll
  for (int rw = 0; rw < 2; ++rw) {
    const u32x4 xv = rw ? xb : xa;
    float v[8] = {bf_lo(xv[0]), bf_hi(xv[0]), bf_lo(xv[1]), bf_hi(xv[1]), bf_lo(xv[2]), bf_hi(xv[2]), bf_lo(xv[3]), bf_hi(xv[3])};
    const float mean = wave_sum(((v[0] + v[1]) + (v[2] + v[3])) + ((v[4] + v[5]) + (v[6] + v[7]))) * (1.0f / DM);
    float ss = 0.f;
#pragma unroll
    for (int j = 0; j < 8; ++j) { const float d = v[j] - mean; ss = fmaf(d, d, ss); }
    const float var = wave_sum(ss) * (1.0f / DM);
    const float mb = bfr(mean), den = bfr(sqrtf(bfr(bfr(var) + 1e-5f)));
    const float rd = 1.0f / den;
    float y[8];
#pragma unroll
    for (int j = 0; j < 8; ++j) {
      const float d = bfr(v[j] - mb), q0 = d * rd;
      y[j] = fmaf(fmaf(-q0, den, d), rd, q0);
    }
    *(LAS u32x4*)(lds + L_XN + (2 * xw + rw) * XN_STRIDE + lane * 16) =
        u32x4{pack_bf(y[0], y[1]), pack_bf(y[2], y[3]), pack_bf(y[4], y[5]), pack_bf(y[6], y[7])};
  }
  STAMP(9);
  aux_sync(lds + L_CNT + 0, lane);
  const u32 xfrag = lds + L_XN + (r & 7) * XN_STRIDE + 256 * xw + 16 * g;
  const u32 bfrag = stg + (8 * g + (r >> 2)) * 32 + 8 * (r & 3);
  const u32 redw = lds + L_RED + xw * 512 + r * 32 + g * 16;
  const u64 tag = (epoch + 1ull) & 0xFFFFFFFFFFFFull;
  const u32 tlo = (u32)tag & 0xffffu, thi = (u32)(tag >> 16);
  wait_vm<0>();
  STAMP(10);
  {
    f32x4 c = {0.f, 0.f, 0.f, 0.f};
#pragma unroll
    for (int i = 0; i < 4; ++i) c = mfma(lds_rd128(xfrag + 64 * i), stage_frag(bfrag, i), c);
    if (g < 2) *(LAS f32x4*)redw = c;
  }
  aux_sync(lds + L_CNT + 4, lane);
  if (g < 2) {
    const u32 ra = lds + L_RED + r * 32 + g * 16 + xw * 4;
    const float qv = (*(const LAS float*)ra + *(const LAS float*)(ra + 512)) + (*(const LAS float*)(ra + 1024) + *(const LAS float*)(ra + 1536));
    __hip_atomic_store((GAS u64*)(ws + WS_GQ) + ((kvh * 8 + 4 * g + xw) * 512 + rr * 16 + r),
                       (tag << 16) | (pack_bf(qv, 0.f) & 0xffffu), __ATOMIC_RELAXED, __HIP_MEMORY_SCOPE_AGENT);
  }
  STAMP(11);
  {
    const u32 qa = WS_GQ + ((kvh * 8 + b) * 512 + xw * 128 + 2 * lane) * 8;
    u32x4 v;
#pragma unroll 1
    for (u32 spin = 0; spin < SPIN_CAP; ++spin) {
      asm volatile("" ::: "memory");
      v = __builtin_amdgcn_raw_buffer_load_b128(wsr, qa, 0, 16);
      if (all_lanes(((v[0] >> 16) == tlo) & (v[1] == thi) & ((v[2] >> 16) == tlo) & (v[3] == thi))) break;
    }
    *(LAS u32*)(lds + L_Q + xw * Q_STRIDE + lane * 4) = (v[0] & 0xffffu) | (v[2] << 16);
  }
  STAMP(2);
  BARRIER();

  const int hh = kvh * 4 + (rr >> 3), nj = rr & 7;
  const u32 g1_out = WS_G1 + (((kvh * 4 + xw) * 8 + b) * 4 + sp) * G1_SLOT;
  {
    const u32x4 z = {0u, 0u, 0u, 0u};
    __builtin_amdgcn_raw_buffer_store_b128(z, wsr, g1_out + lane * 16, 0, 16);
    if (lane == 0) __builtin_amdgcn_raw_buffer_store_b128(z, wsr, g1_out + 1024, 0, 16);
    __builtin_amdgcn_raw_buffer_store_b128(z, wsr, WS_G2 + (((lane >> 3) * 32 + 4 * nj + xw) * 32 + hh) * 128 + (lane & 7) * 16, 0, 16);
  }
  const u32 xres = ((const GAS u32*)x_)[b * 256 + (kvh * 4 + sp) * 8 + (lane & 7)];
  const u32 vseg1k = (lane >> 1) * 1024 + (lane & 1) * 16;
  {
    const char* src = wo + (size_t)(hh * 128) * 1024 + (64 * nj + 16 * xw) * 2;
#pragma unroll
    for (int j = 0; j < 4; ++j) dma1(stg + j * 1024, src + j * 32 * 1024, vseg1k);
  }
  wait_vm<0>();
  aux_sync(lds + L_CNT + 8, lane);
  if (tid == 256) __hip_atomic_store((GAS u64*)(ws + WS_RDY) + wg_id, tag << 16, __ATOMIC_RELAXED, __HIP_MEMORY_SCOPE_AGENT);
  u32x4 bfo[4], bfd[2];
#pragma unroll
  for (int i = 0; i < 4; ++i) bfo[i] = stage_frag(bfrag, i);
  WAIT_LGKM0();
  {
#pragma unroll
    for (int j = 0; j < 2; ++j) {
      const int row = min(56 * hh + 32 * j + (lane >> 1), FF - 1);
      dma1(stg + j * 1024, wd + (64 * nj + 16 * xw) * 2, row * 1024 + (lane & 1) * 16);
    }
  }
  wait_vm<0>();
#pragma unroll
  for (int i = 0; i < 2; ++i) bfd[i] = stage_frag(bfrag, i);
  WAIT_LGKM0();
  STAMP(12);
  if (rr < 14) {
    const int ft = kvh * 14 + rr;
    const u32 vsegf = (lane >> 1) * (FF * 2) + (lane & 1) * 16;
    f32x4 cg = {0.f, 0.f, 0.f, 0.f}, cu = {0.f, 0.f, 0.f, 0.f};
#pragma unroll
    for (int j = 0; j < 4; ++j) dma1(stg + j * 1024, wg + (size_t)(128 * xw + 32 * j) * (FF * 2) + ft * 32, vsegf);
    wait_vm<0>();
#pragma unroll
    for (int i = 0; i < 4; ++i) cg = mfma(lds_rd128(xfrag + 64 * i), stage_frag(bfrag, i), cg);
    WAIT_LGKM0();
#pragma unroll
    for (int j = 0; j < 4; ++j) dma1(stg + j * 1024, wu + (size_t)(128 * xw + 32 * j) * (FF * 2) + ft * 32, vsegf);
    wait_vm<0>();
#pragma unroll
    for (int i = 0; i < 4; ++i) cu = mfma(lds_rd128(xfrag + 64 * i), stage_frag(bfrag, i), cu);
    if (g < 2) { *(LAS f32x4*)redw = cg; *(LAS f32x4*)(redw + 2048) = cu; }
    aux_sync(lds + L_CNT + 12, lane);
    if (g < 2) {
      const u32 ra = lds + L_RED + r * 32 + g * 16 + xw * 4;
      const float gv = (*(const LAS float*)ra + *(const LAS float*)(ra + 512)) + (*(const LAS float*)(ra + 1024) + *(const LAS float*)(ra + 1536));
      const float uv = (*(const LAS float*)(ra + 2048) + *(const LAS float*)(ra + 2560)) + (*(const LAS float*)(ra + 3072) + *(const LAS float*)(ra + 3584));
      __hip_atomic_store((GAS u64*)(ws + WS_GH) + ((4 * g + xw) * FF + ft * 16 + r),
                         (tag << 16) | (pack_bf(bfr(gv) * bfr(uv), 0.f) & 0xffffu), __ATOMIC_RELAXED, __HIP_MEMORY_SCOPE_AGENT);
    }
  }
  STAMP(13);
  f32x4 acc = {0.f, 0.f, 0.f, 0.f};
  {
    const int hb = (xw * 64 + lane) >> 5, hp = lane & 31;
    const u32 ha = WS_GH + (hb * FF + 56 * hh + 2 * min(hp, 27)) * 8;
    u32x4 v;
#pragma unroll 1
    for (u32 spin = 0; spin < SPIN_CAP; ++spin) {
      asm volatile("" ::: "memory");
      v = __builtin_amdgcn_raw_buffer_load_b128(wsr, ha, 0, 16);
      if (all_lanes(((v[0] >> 16) == tlo) & (v[1] == thi) & ((v[2] >> 16) == tlo) & (v[3] == thi))) break;
      __builtin_amdgcn_s_sleep(8);
    }
    *(LAS u32*)(lds + L_H + hb * H_STRIDE + hp * 4) = hp < 28 ? ((v[0] & 0xffffu) | (v[2] << 16)) : 0u;
    aux_sync(lds + L_CNT + 16, lane);
#pragma unroll
    for (int i = 0; i < 2; ++i) acc = mfma(lds_rd128(lds + L_H + (r & 7) * H_STRIDE + 64 * i + 16 * g), bfd[i], acc);
  }
  STAMP(14);
  {
#pragma unroll 1
    for (u32 spin = 0; spin < SPIN_CAP; ++spin) {
      asm volatile("" ::: "memory");
      bool ok = true;
#pragma unroll
      for (int m = 0; m < 2; ++m) {
        const u32x4 v = __builtin_amdgcn_raw_buffer_load_b128(wsr, WS_RDY + m * 1024 + lane * 16, 0, 16);
        ok = ok & ((v[0] >> 16) == tlo) & (v[1] == thi) & ((v[2] >> 16) == tlo) & (v[3] == thi);
      }
      if (all_lanes(ok)) break;
      __builtin_amdgcn_s_sleep(8);
    }
  }
  STAMP(3);
  BARRIER();
  STAMP(4);
  {
    float mw[4], lw[4]; f32x2 ow[4];
#pragma unroll
    for (int w = 0; w < 4; ++w) {
      const f32x2 ml = *(const LAS f32x2*)(lds + L_RING + w * 32768 + MRG_ML + xw * 8);
      mw[w] = ml[0]; lw[w] = ml[1];
      ow[w] = *(const LAS f32x2*)(lds + L_RING + w * 32768 + xw * 528 + lane * 8);
    }
    const float M = fmaxf(fmaxf(mw[0], mw[1]), fmaxf(mw[2], mw[3]));
    float L = 0.f, o0 = 0.f, o1 = 0.f;
#pragma unroll
    for (int w = 0; w < 4; ++w) {
      const float e = __builtin_amdgcn_exp2f((mw[w] - M) * LOG2E);
      L = fmaf(e, lw[w], L); o0 = fmaf(e, ow[w][0], o0); o1 = fmaf(e, ow[w][1], o1);
    }
    __builtin_amdgcn_raw_buffer_store_b128(u32x4{u_of(o0), 1u, u_of(o1), 1u}, wsr, g1_out + lane * 16, 0, 16);
    if (lane == 0) __builtin_amdgcn_raw_buffer_store_b128(u32x4{u_of(M), 1u, u_of(L), 1u}, wsr, g1_out + 1024, 0, 16);
  }
  {
    const u32 g1_in = WS_G1 + ((hh * 8 + 2 * xw) * 4) * G1_SLOT;
    u32x4 d[8], ml;
#pragma unroll 1
    for (u32 spin = 0; spin < SPIN_CAP; ++spin) {
      asm volatile("" ::: "memory");
      ml = __builtin_amdgcn_raw_buffer_load_b128(wsr, g1_in + (lane & 7) * G1_SLOT + 1024, 0, 16);
#pragma unroll
      for (int s = 0; s < 8; ++s) d[s] = __builtin_amdgcn_raw_buffer_load_b128(wsr, g1_in + s * G1_SLOT + lane * 16, 0, 16);
      bool ok = (ml[1] == 1u) & (ml[3] == 1u);
#pragma unroll
      for (int s = 0; s < 8; ++s) ok = ok & (d[s][1] == 1u) & (d[s][3] == 1u);
      if (all_lanes(ok)) break;
    }
    STAMP(5);
#pragma unroll
    for (int rw = 0; rw < 2; ++rw) {
      float ms[4], ls[4];
#pragma unroll
      for (int s = 0; s < 4; ++s) {
        ms[s] = f_of(__builtin_amdgcn_readlane(ml[0], 4 * rw + s));
        ls[s] = f_of(__builtin_amdgcn_readlane(ml[2], 4 * rw + s));
      }
      const float M = fmaxf(fmaxf(ms[0], ms[1]), fmaxf(ms[2], ms[3]));
      float L = 0.f, o0 = 0.f, o1 = 0.f;
#pragma unroll
      for (int s = 0; s < 4; ++s) {
        const float e = __builtin_amdgcn_exp2f((ms[s] - M) * LOG2E);
        L = fmaf(e, ls[s], L); o0 = fmaf(e, f_of(d[4 * rw + s][0]), o0); o1 = fmaf(e, f_of(d[4 * rw + s][2]), o1);
      }
      const float inv = 1.0f / L;
      *(LAS u32*)(lds + L_O + (2 * xw + rw) * Q_STRIDE + lane * 4) = pack_bf(o0 * inv, o1 * inv);
    }
  }
  BARRIER();
#pragma unroll
  for (int i = 0; i < 4; ++i) acc = mfma(lds_rd128(lds + L_O + (r & 7) * Q_STRIDE + 64 * i + 16 * g), bfo[i], acc);
  if (g < 2) {
#pragma unroll
    for (int j = 0; j < 4; ++j)
      __builtin_amdgcn_raw_buffer_store_b64(u32x2{u_of(acc[j]), 1u}, wsr,
                                            WS_G2 + (((4 * g + j) * 32 + 4 * nj + xw) * 32 + hh) * 128 + r * 8, 0, 16);
  }
  if (xw != 0) return;
  {
    const u32 g2_in = WS_G2 + (b * 32 + kvh * 4 + sp) * 4096;
    u32x4 d[4];
#pragma unroll 1
    for (u32 spin = 0; spin < SPIN_CAP; ++spin) {
      asm volatile("" ::: "memory");
      bool ok = true;
#pragma unroll
      for (int m = 0; m < 4; ++m) d[m] = __builtin_amdgcn_raw_buffer_load_b128(wsr, g2_in + m * 1024 + lane * 16, 0, 16);
#pragma unroll
      for (int m = 0; m < 4; ++m) ok = ok & (d[m][1] == 1u) & (d[m][3] == 1u);
      if (all_lanes(ok)) break;
    }
    STAMP(6);
    float v0 = (f_of(d[0][0]) + f_of(d[1][0])) + (f_of(d[2][0]) + f_of(d[3][0]));
    float v1 = (f_of(d[0][2]) + f_of(d[1][2])) + (f_of(d[2][2]) + f_of(d[3][2]));
    v0 += dpp<ROR8>(v0); v1 += dpp<ROR8>(v1);
    v0 = rows_sum(v0); v1 = rows_sum(v1);
    if (lane < 8) *(GAS u32*)(out + b * 256 + (kvh * 4 + sp) * 8 + lane) = pack_bf(bf_lo(xres) + v0, bf_hi(xres) + v1);
    if (wg_id == 0 && lane == 0)
      __hip_atomic_store((GAS u64*)(ws + WS_EPOCH), epoch + 1ull, __ATOMIC_RELAXED, __HIP_MEMORY_SCOPE_AGENT);
    STAMP(7);
  }
}

extern "C" void kernel_launch(void* const* d_in, const int* in_sizes, int n_in, void* d_out, int out_size, void* d_ws,
                              size_t ws_size, hipStream_t stream) {
  (void)in_sizes; (void)n_in; (void)out_size; (void)ws_size;
  hipLaunchKernelGGL(fused_decode_kernel, dim3(NWG), dim3(NTHR), 0, stream, (const u32*)d_in[0], (const char*)d_in[1],
                     (const char*)d_in[2], (const char*)d_in[3], (const char*)d_in[4], (const char*)d_in[6],
                     (const char*)d_in[7], (const char*)d_in[8], (u32*)d_out, (char*)d_ws);
}
```

```cpp
#include <hip/hip_runtime.h>
#include <hip/hip_bf16.h>
#include <stdint.h>
#pragma clang diagnostic ignored "-Wint-to-pointer-cast"

#define GAS __attribute__((address_space(1)))
#define LAS __attribute__((address_space(3)))
typedef unsigned u32;
typedef unsigned long long u64;
typedef u32 u32x2 __attribute__((ext_vector_type(2)));
typedef u32 u32x4 __attribute__((ext_vector_type(4)));
typedef float f32x2 __attribute__((ext_vector_type(2)));
typedef float f32x4 __attribute__((ext_vector_type(4)));
typedef __bf16 bf16x2 __attribute__((ext_vector_type(2)));
typedef __bf16 bf16x8 __attribute__((ext_vector_type(8)));
typedef short s16x4 __attribute__((ext_vector_type(4)));

#define K_NT 1
#define V_NT 1
#define WIN 12
#define ROT 1

namespace {

constexpr int DM = 512, SK = 8192, DH = 128, FF = 1792;
constexpr int NWG = 256, NTHR = 512;
constexpr int KEYS_PER_WAVE = SK / 16;
constexpr int NSTEP = KEYS_PER_WAVE / 32;
constexpr u32 SPIN_CAP = 1u << 18;

constexpr u32 WS_EPOCH = 0;
constexpr u32 WS_RDY = 4096;
constexpr u32 WS_GQ = 8192;
constexpr u32 WS_GH = WS_GQ + 8 * 8 * 512 * 8;
constexpr u32 G1_SLOT = 1152;
constexpr u32 WS_G1 = WS_GH + 8 * FF * 8;
constexpr u32 WS_G2 = WS_G1 + 8 * 4 * 8 * 4 * G1_SLOT;
constexpr u32 WS_END = WS_G2 + 8 * 32 * 32 * 128;
constexpr u32 WS_STAMP = 4u << 20;

constexpr u32 L_RING = 0;
constexpr u32 L_STG = 131072;
constexpr u32 L_XN = L_STG + 16384;
constexpr u32 XN_STRIDE = 1056;
constexpr u32 L_Q = L_XN + 8 * XN_STRIDE;
constexpr u32 Q_STRIDE = 272;
constexpr u32 L_RED = L_Q + 4 * Q_STRIDE;
constexpr u32 L_O = L_RED;
constexpr u32 L_H = L_RED + 4096;
constexpr u32 H_STRIDE = 144;
constexpr u32 L_CNT = L_H + 8 * H_STRIDE;
constexpr u32 L_END = L_CNT + 64;
constexpr u32 MRG_ML = 4 * 528;
static_assert(L_END <= 163840, "LDS");

constexpr float LOG2E = 1.4426950408889634f;
constexpr float QK_SCALE = 0.08838834764831845f;

__device__ __forceinline__ float bf_lo(u32 w) { return __builtin_bit_cast(float, w << 16); }
__device__ __forceinline__ float bf_hi(u32 w) { return __builtin_bit_cast(float, w & 0xffff0000u); }
__device__ __forceinline__ u32 pack_bf(float a, float b) {
  f32x2 v = {a, b};
  return __builtin_bit_cast(u32, __builtin_convertvector(v, bf16x2));
}
__device__ __forceinline__ float bfr(float a) { return bf_lo(pack_bf(a, a)); }
template <int CTRL> __device__ __forceinline__ float dpp(float x) {
  return __builtin_bit_cast(float, __builtin_amdgcn_mov_dpp(__builtin_bit_cast(int, x), CTRL, 0xf, 0xf, true));
}
constexpr int XOR1 = 0xB1, XOR2 = 0x4E, HMIRROR = 0x141, MIRROR = 0x140, ROR8 = 0x128;
__device__ __forceinline__ float f_of(u32 x) { return __builtin_bit_cast(float, x); }
__device__ __forceinline__ u32 u_of(float x) { return __builtin_bit_cast(u32, x); }
__device__ __forceinline__ float rows_max(float x) {
  auto s = __builtin_amdgcn_permlane16_swap(u_of(x), u_of(x), false, false);
  x = fmaxf(f_of(s[0]), f_of(s[1]));
  auto t = __builtin_amdgcn_permlane32_swap(u_of(x), u_of(x), false, false);
  return fmaxf(f_of(t[0]), f_of(t[1]));
}
__device__ __forceinline__ float rows_sum(float x) {
  auto s = __builtin_amdgcn_permlane16_swap(u_of(x), u_of(x), false, false);
  x = f_of(s[0]) + f_of(s[1]);
  auto t = __builtin_amdgcn_permlane32_swap(u_of(x), u_of(x), false, false);
  return f_of(t[0]) + f_of(t[1]);
}
__device__ __forceinline__ float wave_sum(float x) {
  x += dpp<XOR1>(x); x += dpp<XOR2>(x); x += dpp<HMIRROR>(x); x += dpp<MIRROR>(x);
  return rows_sum(x);
}

template <bool NT>
__device__ __forceinline__ void dma4(u32 ldsa, const void* sb, u32 v0, u32 v1, u32 v2, u32 v3) {
  u32 keep;
  if constexpr (NT)
  asm volatile("s_mov_b32 %0, m0\n\ts_mov_b32 m0, %1\n\ts_nop 0\n\t"
               "global_load_lds_dwordx4 %3, %2 nt\n\t"
               "global_load_lds_dwordx4 %4, %2 offset:1024 nt\n\t"
               "global_load_lds_dwordx4 %5, %2 offset:2048 nt\n\t"
               "global_load_lds_dwordx4 %6, %2 offset:3072 nt\n\t"
               "s_mov_b32 m0, %0"
               : "=&s"(keep) : "s"(ldsa), "s"(sb), "v"(v0), "v"(v1), "v"(v2), "v"(v3) : "memory");
  else
  asm volatile("s_mov_b32 %0, m0\n\ts_mov_b32 m0, %1\n\ts_nop 0\n\t"
               "global_load_lds_dwordx4 %3, %2\n\t"
               "global_load_lds_dwordx4 %4, %2 offset:1024\n\t"
               "global_load_lds_dwordx4 %5, %2 offset:2048\n\t"
               "global_load_lds_dwordx4 %6, %2 offset:3072\n\t"
               "s_mov_b32 m0, %0"
               : "=&s"(keep) : "s"(ldsa), "s"(sb), "v"(v0), "v"(v1), "v"(v2), "v"(v3) : "memory");
}
__device__ __forceinline__ void dma1(u32 ldsa, const void* sb, u32 v0) {
  u32 keep;
  asm volatile("s_mov_b32 %0, m0\n\ts_mov_b32 m0, %1\n\ts_nop 0\n\t"
               "global_load_lds_dwordx4 %3, %2\n\t"
               "s_mov_b32 m0, %0"
               : "=&s"(keep) : "s"(ldsa), "s"(sb), "v"(v0) : "memory");
}
#define WAIT_LGKM0() asm volatile("s_waitcnt lgkmcnt(0)" ::: "memory")
#define BARRIER() do { asm volatile("s_waitcnt lgkmcnt(0)" ::: "memory"); __builtin_amdgcn_s_barrier(); asm volatile("" ::: "memory"); } while (0)
template <int N> __device__ __forceinline__ void wait_vm() { asm volatile("s_waitcnt vmcnt(%0)" :: "i"(N) : "memory"); }

__device__ __forceinline__ u32x4 lds_rd128(u32 a) { return *(const LAS u32x4*)a; }
__device__ __forceinline__ u32x2 lds_tr(u32 a) {
  return __builtin_bit_cast(u32x2, __builtin_amdgcn_ds_read_tr16_b64_v4i16((LAS s16x4*)a));
}
__device__ __forceinline__ f32x4 mfma(u32x4 a, u32x4 b, f32x4 c) {
  return __builtin_amdgcn_mfma_f32_16x16x32_bf16(__builtin_bit_cast(bf16x8, a), __builtin_bit_cast(bf16x8, b), c, 0, 0, 0);
}
__device__ __forceinline__ u32x4 stage_frag(u32 ba, int i) {
  const u32x2 lo = lds_tr(ba + i * 1024), hi = lds_tr(ba + i * 1024 + 128);
  return u32x4{lo[0], lo[1], hi[0], hi[1]};
}
__device__ __forceinline__ void aux_sync(u32 cnt, int lane) {
  WAIT_LGKM0();
  if (lane == 0) __hip_atomic_fetch_add((LAS u32*)cnt, 1u, __ATOMIC_RELAXED, __HIP_MEMORY_SCOPE_WORKGROUP);
#pragma unroll 1
  for (u32 spin = 0; spin < SPIN_CAP; ++spin) {
    if (__builtin_amdgcn_readfirstlane(*(volatile LAS u32*)cnt) >= 4u) break;
  }
  asm volatile("" ::: "memory");
}
__device__ __forceinline__ bool all_lanes(bool ok) { return __builtin_amdgcn_ballot_w64(!ok) == 0ull; }

#define STAMP(i) do { } while (0)

struct StreamState {
  f32x4 acc[8];
  float m, l;
};

__device__ __forceinline__ void issue_k(u32 ldsa, const char* src, const u32 (&vk)[4]) {
  wait_vm<WIN - 4>();
  dma4<K_NT>(ldsa, src, vk[0], vk[1], vk[2], vk[3]);
}
__device__ __forceinline__ void issue_v(u32 ldsa, const char* src, const u32 (&vv)[2]) {
  wait_vm<WIN - 4>();
  dma4<V_NT>(ldsa, src, vv[0], vv[1], vv[0], vv[1]);
}

template <int SLOT, int WK, int WV, bool ISSUE>
__device__ __forceinline__ void stream_step(StreamState& st, const u32x4 (&qf)[4], const u32 (&ka)[4], const u32 (&va)[8],
                                            u32 ring, const char* kn, const char* vn, const u32 (&vk)[4], const u32 (&vv)[2]) {
  wait_vm<WK>();
  u32x4 kf[2][4];
#pragma unroll
  for (int tt = 0; tt < 2; ++tt)
#pragma unroll
    for (int i = 0; i < 4; ++i) kf[tt][i] = lds_rd128(ka[i] + SLOT * 16384 + tt * 4096);
  WAIT_LGKM0();
  if constexpr (ISSUE) {
    issue_k(ring + SLOT * 16384, kn, vk);
    issue_k(ring + SLOT * 16384 + 4096, kn + 4096, vk);
  }
  f32x4 s0 = {0.f, 0.f, 0.f, 0.f}, s1 = {0.f, 0.f, 0.f, 0.f};
#pragma unroll
  for (int i = 0; i < 4; ++i) { s0 = mfma(kf[0][i], qf[i], s0); s1 = mfma(kf[1][i], qf[i], s1); }
  float t[8];
  {
    const u32 a = pack_bf(s0[0], s0[1]), b = pack_bf(s0[2], s0[3]), c = pack_bf(s1[0], s1[1]), d = pack_bf(s1[2], s1[3]);
    const u32 a2 = pack_bf(bf_lo(a) * QK_SCALE, bf_hi(a) * QK_SCALE), b2 = pack_bf(bf_lo(b) * QK_SCALE, bf_hi(b) * QK_SCALE);
    const u32 c2 = pack_bf(bf_lo(c) * QK_SCALE, bf_hi(c) * QK_SCALE), d2 = pack_bf(bf_lo(d) * QK_SCALE, bf_hi(d) * QK_SCALE);
    t[0] = bf_lo(a2); t[1] = bf_hi(a2); t[2] = bf_lo(b2); t[3] = bf_hi(b2);
    t[4] = bf_lo(c2); t[5] = bf_hi(c2); t[6] = bf_lo(d2); t[7] = bf_hi(d2);
  }
  float mx = fmaxf(fmaxf(fmaxf(t[0], t[1]), fmaxf(t[2], t[3])), fmaxf(fmaxf(t[4], t[5]), fmaxf(t[6], t[7])));
  mx = rows_max(mx);
  if (__builtin_amdgcn_ballot_w64(mx > st.m) != 0ull) {
    const float mn = fmaxf(st.m, mx);
    const float alpha = __builtin_amdgcn_exp2f((st.m - mn) * LOG2E);
    st.m = mn; st.l *= alpha;
#pragma unroll
    for (int c = 0; c < 8; ++c) st.acc[c] *= alpha;
  }
  const float mb = st.m * LOG2E;
  float p[8];
#pragma unroll
  for (int j = 0; j < 8; ++j) p[j] = __builtin_amdgcn_exp2f(fmaf(t[j], LOG2E, -mb));
  st.l += ((p[0] + p[1]) + (p[2] + p[3])) + ((p[4] + p[5]) + (p[6] + p[7]));
  const u32x4 pf = {pack_bf(p[0], p[1]), pack_bf(p[2], p[3]), pack_bf(p[4], p[5]), pack_bf(p[6], p[7])};
  wait_vm<WV>();
  u32x4 vf[8];
#pragma unroll
  for (int c = 0; c < 8; ++c) {
    const u32x2 lo = lds_tr(va[c] + SLOT * 16384 + 8192), hi = lds_tr(va[c] + SLOT * 16384 + 8192 + 4096);
    vf[c] = u32x4{lo[0], lo[1], hi[0], hi[1]};
  }
  WAIT_LGKM0();
  if constexpr (ISSUE) {
    issue_v(ring + SLOT * 16384 + 8192, vn, vv);
    issue_v(ring + SLOT * 16384 + 8192 + 4096, vn + 4096, vv);
  }
#pragma unroll
  for (int c = 0; c < 8; ++c) st.acc[c] = mfma(vf[c], pf, st.acc[c]);
}

}

__global__ void __launch_bounds__(NTHR, 2)
fused_decode_kernel(const u32* __restrict__ x_, const char* __restrict__ kc, const char* __restrict__ vc, const char* __restrict__ wq,
                    const char* __restrict__ wo, const char* __restrict__ wg, const char* __restrict__ wu, const char* __restrict__ wd,
                    u32* __restrict__ out, char* __restrict__ ws) {
  __shared__ __attribute__((aligned(1024))) char smem[L_END];
  const u32 lds = (u32)(size_t)(LAS char*)smem;
  const int tid = threadIdx.x, lane = tid & 63;
  const int wave = __builtin_amdgcn_readfirstlane(tid >> 6);
  const int wg_id = blockIdx.x;
  const int kvh = wg_id & 7, rr = wg_id >> 3, b = rr >> 2, sp = rr & 3;
  const int g = lane >> 4, r = lane & 15;
  STAMP(0);

  if (wave < 4) {
    const size_t seq = ((size_t)(b * 8 + kvh) * SK + (size_t)(sp * 4 + wave) * KEYS_PER_WAVE) * (DH * 2);
    const char* kb = kc + seq;
    const char* vb = vc + seq;
    const u32 ring = lds + L_RING + wave * 32768;
    const int rot = ROT ? ((wg_id * 37 + wave * 11) & (NSTEP - 1)) : 0;
    u32 vk[4], vv[2];
#pragma unroll
    for (int n = 0; n < 4; ++n) vk[n] = g * 256 + (((r ^ g) ^ (4 * n)) << 4);
#pragma unroll
    for (int n = 0; n < 2; ++n) vv[n] = g * 256 + ((r ^ ((4 * n + g) << 1)) << 4);
    BARRIER();
    {
      const u32 o0 = rot * 8192, o1 = ((rot + 1) & (NSTEP - 1)) * 8192;
      issue_k(ring, kb + o0, vk); issue_k(ring + 4096, kb + o0 + 4096, vk);
      issue_v(ring + 8192, vb + o0, vv); issue_v(ring + 8192 + 4096, vb + o0 + 4096, vv);
      issue_k(ring + 16384, kb + o1, vk); issue_k(ring + 16384 + 4096, kb + o1 + 4096, vk);
      issue_v(ring + 16384 + 8192, vb + o1, vv); issue_v(ring + 16384 + 8192 + 4096, vb + o1 + 4096, vv);
    }
    STAMP(1);
    BARRIER();
    STAMP(2);
    u32x4 qf[4];
#pragma unroll
    for (int i = 0; i < 4; ++i) {
      const u32x4 v = lds_rd128(lds + L_Q + (r & 3) * Q_STRIDE + 64 * i + 16 * g);
      qf[i] = r < 4 ? v : u32x4{0u, 0u, 0u, 0u};
    }
    u32 ka[4], va[8];
#pragma unroll
    for (int i = 0; i < 4; ++i) ka[i] = ring + r * 256 + ((((4 * i + g) ^ r)) << 4);
    {
      const int q4 = r >> 2, p4 = r & 3, rho = (4 * g + q4) & 7;
      const u32 base = (4 * g + q4) * 256 + 16 * (p4 >> 1) + 8 * (p4 & 1);
#pragma unroll
      for (int c = 0; c < 8; ++c) va[c] = ring + base + ((c ^ rho) << 5);
    }
    StreamState st;
#pragma unroll
    for (int c = 0; c < 8; ++c) st.acc[c] = f32x4{0.f, 0.f, 0.f, 0.f};
    st.m = -INFINITY; st.l = 0.f;
#pragma unroll 1
    for (int t = 0; t < NSTEP - 2; t += 2) {
      const u32 o0 = ((t + 2 + rot) & (NSTEP - 1)) * 8192, o1 = ((t + 3 + rot) & (NSTEP - 1)) * 8192;
      stream_step<0, 24, 24, true>(st, qf, ka, va, ring, kb + o0, vb + o0, vk, vv);
      stream_step<1, 24, 24, true>(st, qf, ka, va, ring, kb + o1, vb + o1, vk, vv);
    }
    stream_step<0, 24, 16, false>(st, qf, ka, va, ring, kb, vb, vk, vv);
    stream_step<1, 8, 0, false>(st, qf, ka, va, ring, kb, vb, vk, vv);
    STAMP(3);
    const float lsum = rows_sum(st.l);
    if (r < 4) {
#pragma unroll
      for (int c = 0; c < 8; ++c) *(LAS f32x4*)(ring + r * 528 + (16 * c + 4 * g) * 4) = st.acc[c];
      if (g == 0) *(LAS f32x2*)(ring + MRG_ML + r * 8) = f32x2{st.m, lsum};
    }
    BARRIER();
    STAMP(4);
    return;
  }

  __builtin_amdgcn_s_setprio(3);
  const int xw = wave - 4;
  const u32 stg = lds + L_STG + xw * 4096;
  const u32 vseg8k = (lane >> 1) * 8192 + (lane & 1) * 16;
  const u32x4 xa = ((const GAS u32x4*)x_)[(2 * xw) * 64 + lane];
  const u32x4 xb = ((const GAS u32x4*)x_)[(2 * xw + 1) * 64 + lane];
  {
    const char* wsrc = wq + (size_t)(128 * xw) * 8192 + (kvh * 512 + rr * 16) * 2;
#pragma unroll
    for (int j = 0; j < 4; ++j) dma1(stg + j * 1024, wsrc + (size_t)j * 32 * 8192, vseg8k);
  }
  const u64 epoch = __hip_atomic_load((GAS u64*)(ws + WS_EPOCH), __ATOMIC_RELAXED, __HIP_MEMORY_SCOPE_AGENT);
  if (tid < 256 + 16) *(LAS u32*)(lds + L_CNT + (tid - 256) * 4) = 0u;
  BARRIER();
  const __amdgpu_buffer_rsrc_t wsr = __builtin_amdgcn_make_buffer_rsrc(ws, 0, WS_END, 0x00020000);
#pragma unroll
  for (int rw = 0; rw < 2; ++rw) {
    const u32x4 xv = rw ? xb : xa;
    float v[8] = {bf_lo(xv[0]), bf_hi(xv[0]), bf_lo(xv[1]), bf_hi(xv[1]), bf_lo(xv[2]), bf_hi(xv[2]), bf_lo(xv[3]), bf_hi(xv[3])};
    const float mean = wave_sum(((v[0] + v[1]) + (v[2] + v[3])) + ((v[4] + v[5]) + (v[6] + v[7]))) * (1.0f / DM);
    float ss = 0.f;
#pragma unroll
    for (int j = 0; j < 8; ++j) { const float d = v[j] - mean; ss = fmaf(d, d, ss); }
    const float var = wave_sum(ss) * (1.0f / DM);
    const float mb = bfr(mean), den = bfr(sqrtf(bfr(bfr(var) + 1e-5f)));
    const float rd = 1.0f / den;
    float y[8];
#pragma unroll
    for (int j = 0; j < 8; ++j) {
      const float d = bfr(v[j] - mb), q0 = d * rd;
      y[j] = fmaf(fmaf(-q0, den, d), rd, q0);
    }
    *(LAS u32x4*)(lds + L_XN + (2 * xw + rw) * XN_STRIDE + lane * 16) =
        u32x4{pack_bf(y[0], y[1]), pack_bf(y[2], y[3]), pack_bf(y[4], y[5]), pack_bf(y[6], y[7])};
  }
  STAMP(9);
  aux_sync(lds + L_CNT + 0, lane);
  const u32 xfrag = lds + L_XN + (r & 7) * XN_STRIDE + 256 * xw + 16 * g;
  const u32 bfrag = stg + (8 * g + (r >> 2)) * 32 + 8 * (r & 3);
  const u32 redw = lds + L_RED + xw * 512 + r * 32 + g * 16;
  const u64 tag = (epoch + 1ull) & 0xFFFFFFFFFFFFull;
  const u32 tlo = (u32)tag & 0xffffu, thi = (u32)(tag >> 16);
  wait_vm<0>();
  STAMP(10);
  {
    f32x4 c = {0.f, 0.f, 0.f, 0.f};
#pragma unroll
    for (int i = 0; i < 4; ++i) c = mfma(lds_rd128(xfrag + 64 * i), stage_frag(bfrag, i), c);
    if (g < 2) *(LAS f32x4*)redw = c;
  }
  aux_sync(lds + L_CNT + 4, lane);
  if (g < 2) {
    const u32 ra = lds + L_RED + r * 32 + g * 16 + xw * 4;
    const float qv = (*(const LAS float*)ra + *(const LAS float*)(ra + 512)) + (*(const LAS float*)(ra + 1024) + *(const LAS float*)(ra + 1536));
    __hip_atomic_store((GAS u64*)(ws + WS_GQ) + ((kvh * 8 + 4 * g + xw) * 512 + rr * 16 + r),
                       (tag << 16) | (pack_bf(qv, 0.f) & 0xffffu), __ATOMIC_RELAXED, __HIP_MEMORY_SCOPE_AGENT);
  }
  STAMP(11);
  {
    const u32 qa = WS_GQ + ((kvh * 8 + b) * 512 + xw * 128 + 2 * lane) * 8;
    u32x4 v;
#pragma unroll 1
    for (u32 spin = 0; spin < SPIN_CAP; ++spin) {
      asm volatile("" ::: "memory");
      v = __builtin_amdgcn_raw_buffer_load_b128(wsr, qa, 0, 16);
      if (all_lanes(((v[0] >> 16) == tlo) & (v[1] == thi) & ((v[2] >> 16) == tlo) & (v[3] == thi))) break;
    }
    *(LAS u32*)(lds + L_Q + xw * Q_STRIDE + lane * 4) = (v[0] & 0xffffu) | (v[2] << 16);
  }
  STAMP(2);
  BARRIER();

  const int hh = kvh * 4 + (rr >> 3), nj = rr & 7;
  const u32 g1_out = WS_G1 + (((kvh * 4 + xw) * 8 + b) * 4 + sp) * G1_SLOT;
  {
    const u32x4 z = {0u, 0u, 0u, 0u};
    __builtin_amdgcn_raw_buffer_store_b128(z, wsr, g1_out + lane * 16, 0, 16);
    if (lane == 0) __builtin_amdgcn_raw_buffer_store_b128(z, wsr, g1_out + 1024, 0, 16);
    __builtin_amdgcn_raw_buffer_store_b128(z, wsr, WS_G2 + (((lane >> 3) * 32 + 4 * nj + xw) * 32 + hh) * 128 + (lane & 7) * 16, 0, 16);
  }
  const u32 xres = ((const GAS u32*)x_)[b * 256 + (kvh * 4 + sp) * 8 + (lane & 7)];
  const u32 vseg1k = (lane >> 1) * 1024 + (lane & 1) * 16;
  {
    const char* src = wo + (size_t)(hh * 128) * 1024 + (64 * nj + 16 * xw) * 2;
#pragma unroll
    for (int j = 0; j < 4; ++j) dma1(stg + j * 1024, src + j * 32 * 1024, vseg1k);
  }
  wait_vm<0>();
  aux_sync(lds + L_CNT + 8, lane);
  if (tid == 256) __hip_atomic_store((GAS u64*)(ws + WS_RDY) + wg_id, tag << 16, __ATOMIC_RELAXED, __HIP_MEMORY_SCOPE_AGENT);
  u32x4 bfo[4], bfd[2];
#pragma unroll
  for (int i = 0; i < 4; ++i) bfo[i] = stage_frag(bfrag, i);
  WAIT_LGKM0();
  {
#pragma unroll
    for (int j = 0; j < 2; ++j) {
      const int row = min(56 * hh + 32 * j + (lane >> 1), FF - 1);
      dma1(stg + j * 1024, wd + (64 * nj + 16 * xw) * 2, row * 1024 + (lane & 1) * 16);
    }
  }
  wait_vm<0>();
#pragma unroll
  for (int i = 0; i < 2; ++i) bfd[i] = stage_frag(bfrag, i);
  WAIT_LGKM0();
  STAMP(12);
  if (rr < 14) {
    const int ft = kvh * 14 + rr;
    const u32 vsegf = (lane >> 1) * (FF * 2) + (lane & 1) * 16;
    f32x4 cg = {0.f, 0.f, 0.f, 0.f}, cu = {0.f, 0.f, 0.f, 0.f};
#pragma unroll
    for (int j = 0; j < 4; ++j) dma1(stg + j * 1024, wg + (size_t)(128 * xw + 32 * j) * (FF * 2) + ft * 32, vsegf);
    wait_vm<0>();
#pragma unroll
    for (int i = 0; i < 4; ++i) cg = mfma(lds_rd128(xfrag + 64 * i), stage_frag(bfrag, i), cg);
    WAIT_LGKM0();
#pragma unroll
    for (int j = 0; j < 4; ++j) dma1(stg + j * 1024, wu + (size_t)(128 * xw + 32 * j) * (FF * 2) + ft * 32, vsegf);
    wait_vm<0>();
#pragma unroll
    for (int i = 0; i < 4; ++i) cu = mfma(lds_rd128(xfrag + 64 * i), stage_frag(bfrag, i), cu);
    if (g < 2) { *(LAS f32x4*)redw = cg; *(LAS f32x4*)(redw + 2048) = cu; }
    aux_sync(lds + L_CNT + 12, lane);
    if (g < 2) {
      const u32 ra = lds + L_RED + r * 32 + g * 16 + xw * 4;
      const float gv = (*(const LAS float*)ra + *(const LAS float*)(ra + 512)) + (*(const LAS float*)(ra + 1024) + *(const LAS float*)(ra + 1536));
      const float uv = (*(const LAS float*)(ra + 2048) + *(const LAS float*)(ra + 2560)) + (*(const LAS float*)(ra + 3072) + *(const LAS float*)(ra + 3584));
      __hip_atomic_store((GAS u64*)(ws + WS_GH) + ((4 * g + xw) * FF + ft * 16 + r),
                         (tag << 16) | (pack_bf(bfr(gv) * bfr(uv), 0.f) & 0xffffu), __ATOMIC_RELAXED, __HIP_MEMORY_SCOPE_AGENT);
    }
  }
  STAMP(13);
  f32x4 acc = {0.f, 0.f, 0.f, 0.f};
  {
    const int hb = (xw * 64 + lane) >> 5, hp = lane & 31;
    const u32 ha = WS_GH + (hb * FF + 56 * hh + 2 * min(hp, 27)) * 8;
    u32x4 v;
#pragma unroll 1
    for (u32 spin = 0; spin < SPIN_CAP; ++spin) {
      asm volatile("" ::: "memory");
      v = __builtin_amdgcn_raw_buffer_load_b128(wsr, ha, 0, 16);
      if (all_lanes(((v[0] >> 16) == tlo) & (v[1] == thi) & ((v[2] >> 16) == tlo) & (v[3] == thi))) break;
      __builtin_amdgcn_s_sleep(8);
    }
    *(LAS u32*)(lds + L_H + hb * H_STRIDE + hp * 4) = hp < 28 ? ((v[0] & 0xffffu) | (v[2] << 16)) : 0u;
    aux_sync(lds + L_CNT + 16, lane);
#pragma unroll
    for (int i = 0; i < 2; ++i) acc = mfma(lds_rd128(lds + L_H + (r & 7) * H_STRIDE + 64 * i + 16 * g), bfd[i], acc);
  }
  STAMP(14);
  {
#pragma unroll 1
    for (u32 spin = 0; spin < SPIN_CAP; ++spin) {
      asm volatile("" ::: "memory");
      bool ok = true;
#pragma unroll
      for (int m = 0; m < 2; ++m) {
        const u32x4 v = __builtin_amdgcn_raw_buffer_load_b128(wsr, WS_RDY + m * 1024 + lane * 16, 0, 16);
        ok = ok & ((v[0] >> 16) == tlo) & (v[1] == thi) & ((v[2] >> 16) == tlo) & (v[3] == thi);
      }
      if (all_lanes(ok)) break;
      __builtin_amdgcn_s_sleep(8);
    }
  }
  STAMP(3);
  BARRIER();
  STAMP(4);
  {
    float mw[4], lw[4]; f32x2 ow[4];
#pragma unroll
    for (int w = 0; w < 4; ++w) {
      const f32x2 ml = *(const LAS f32x2*)(lds + L_RING + w * 32768 + MRG_ML + xw * 8);
      mw[w] = ml[0]; lw[w] = ml[1];
      ow[w] = *(const LAS f32x2*)(lds + L_RING + w * 32768 + xw * 528 + lane * 8);
    }
    const float M = fmaxf(fmaxf(mw[0], mw[1]), fmaxf(mw[2], mw[3]));
    float L = 0.f, o0 = 0.f, o1 = 0.f;
#pragma unroll
    for (int w = 0; w < 4; ++w) {
      const float e = __builtin_amdgcn_exp2f((mw[w] - M) * LOG2E);
      L = fmaf(e, lw[w], L); o0 = fmaf(e, ow[w][0], o0); o1 = fmaf(e, ow[w][1], o1);
    }
    __builtin_amdgcn_raw_buffer_store_b128(u32x4{u_of(o0), 1u, u_of(o1), 1u}, wsr, g1_out + lane * 16, 0, 16);
    if (lane == 0) __builtin_amdgcn_raw_buffer_store_b128(u32x4{u_of(M), 1u, u_of(L), 1u}, wsr, g1_out + 1024, 0, 16);
  }
  {
    const u32 g1_in = WS_G1 + ((hh * 8 + 2 * xw) * 4) * G1_SLOT;
    u32x4 d[8], ml;
#pragma unroll 1
    for (u32 spin = 0; spin < SPIN_CAP; ++spin) {
      asm volatile("" ::: "memory");
      ml = __builtin_amdgcn_raw_buffer_load_b128(wsr, g1_in + (lane & 7) * G1_SLOT + 1024, 0, 16);
#pragma unroll
      for (int s = 0; s < 8; ++s) d[s] = __builtin_amdgcn_raw_buffer_load_b128(wsr, g1_in + s * G1_SLOT + lane * 16, 0, 16);
      bool ok = (ml[1] == 1u) & (ml[3] == 1u);
#pragma unroll
      for (int s = 0; s < 8; ++s) ok = ok & (d[s][1] == 1u) & (d[s][3] == 1u);
      if (all_lanes(ok)) break;
    }
    STAMP(5);
#pragma unroll
    for (int rw = 0; rw < 2; ++rw) {
      float ms[4], ls[4];
#pragma unroll
      for (int s = 0; s < 4; ++s) {
        ms[s] = f_of(__builtin_amdgcn_readlane(ml[0], 4 * rw + s));
        ls[s] = f_of(__builtin_amdgcn_readlane(ml[2], 4 * rw + s));
      }
      const float M = fmaxf(fmaxf(ms[0], ms[1]), fmaxf(ms[2], ms[3]));
      float L = 0.f, o0 = 0.f, o1 = 0.f;
#pragma unroll
      for (int s = 0; s < 4; ++s) {
        const float e = __builtin_amdgcn_exp2f((ms[s] - M) * LOG2E);
        L = fmaf(e, ls[s], L); o0 = fmaf(e, f_of(d[4 * rw + s][0]), o0); o1 = fmaf(e, f_of(d[4 * rw + s][2]), o1);
      }
      const float inv = 1.0f / L;
      *(LAS u32*)(lds + L_O + (2 * xw + rw) * Q_STRIDE + lane * 4) = pack_bf(o0 * inv, o1 * inv);
    }
  }
  BARRIER();
#pragma unroll
  for (int i = 0; i < 4; ++i) acc = mfma(lds_rd128(lds + L_O + (r & 7) * Q_STRIDE + 64 * i + 16 * g), bfo[i], acc);
  if (g < 2) {
#pragma unroll
    for (int j = 0; j < 4; ++j)
      __builtin_amdgcn_raw_buffer_store_b64(u32x2{u_of(acc[j]), 1u}, wsr,
                                            WS_G2 + (((4 * g + j) * 32 + 4 * nj + xw) * 32 + hh) * 128 + r * 8, 0, 16);
  }
  if (xw != 0) return;
  {
    const u32 g2_in = WS_G2 + (b * 32 + kvh * 4 + sp) * 4096;
    u32x4 d[4];
#pragma unroll 1
    for (u32 spin = 0; spin < SPIN_CAP; ++spin) {
      asm volatile("" ::: "memory");
      bool ok = true;
#pragma unroll
      for (int m = 0; m < 4; ++m) d[m] = __builtin_amdgcn_raw_buffer_load_b128(wsr, g2_in + m * 1024 + lane * 16, 0, 16);
#pragma unroll
      for (int m = 0; m < 4; ++m) ok = ok & (d[m][1] == 1u) & (d[m][3] == 1u);
      if (all_lanes(ok)) break;
    }
    STAMP(6);
    float v0 = (f_of(d[0][0]) + f_of(d[1][0])) + (f_of(d[2][0]) + f_of(d[3][0]));
    float v1 = (f_of(d[0][2]) + f_of(d[1][2])) + (f_of(d[2][2]) + f_of(d[3][2]));
    v0 += dpp<ROR8>(v0); v1 += dpp<ROR8>(v1);
    v0 = rows_sum(v0); v1 = rows_sum(v1);
    if (lane < 8) *(GAS u32*)(out + b * 256 + (kvh * 4 + sp) * 8 + lane) = pack_bf(bf_lo(xres) + v0, bf_hi(xres) + v1);
    if (wg_id == 0 && lane == 0)
      __hip_atomic_store((GAS u64*)(ws + WS_EPOCH), epoch + 1ull, __ATOMIC_RELAXED, __HIP_MEMORY_SCOPE_AGENT);
    STAMP(7);
  }
}

extern "C" void kernel_launch(void* const* d_in, const int* in_sizes, int n_in, void* d_out, int out_size, void* d_ws,
                              size_t ws_size, hipStream_t stream) {
  (void)in_sizes; (void)n_in; (void)out_size; (void)ws_size;
  hipLaunchKernelGGL(fused_decode_kernel, dim3(NWG), dim3(NTHR), 0, stream, (const u32*)d_in[0], (const char*)d_in[1],
                     (const char*)d_in[2], (const char*)d_in[3], (const char*)d_in[4], (const char*)d_in[6],
                     (const char*)d_in[7], (const char*)d_in[8], (u32*)d_out, (char*)d_ws);
}
```

```cpp
#include <hip/hip_runtime.h>
#include <hip/hip_bf16.h>
#include <stdint.h>
#pragma clang diagnostic ignored "-Wint-to-pointer-cast"

#define GAS __attribute__((address_space(1)))
#define LAS __attribute__((address_space(3)))
typedef unsigned u32;
typedef unsigned long long u64;
typedef u32 u32x2 __attribute__((ext_vector_type(2)));
typedef u32 u32x4 __attribute__((ext_vector_type(4)));
typedef float f32x2 __attribute__((ext_vector_type(2)));
typedef float f32x4 __attribute__((ext_vector_type(4)));
typedef __bf16 bf16x2 __attribute__((ext_vector_type(2)));
typedef __bf16 bf16x8 __attribute__((ext_vector_type(8)));
typedef short s16x4 __attribute__((ext_vector_type(4)));

#define CODE_PF 16256
#define WIN 12
#define PWIN 6
#define TAU 246
#define ASLEEP0 4
#define QSLEEP0 60
#define HSLEEP0 60
#define PSLEEP 12
#define FTRIG 8
#define DTRIG 12
#define ROT 7

namespace {

constexpr int DM = 512, SK = 8192, DH = 128, FF = 1792;
constexpr int NWG = 256, NTHR = 512;
constexpr int NSTEP = SK / (32 * 16);
constexpr u32 SPIN_CAP = 1u << 18;

constexpr u32 WS_EPOCH = 0;
constexpr u32 WS_G1M = 4096;
constexpr u32 WS_GQ = 32768;
constexpr u32 WS_GF = WS_GQ + 8 * 4 * 8 * 512 * 8;
constexpr u32 WS_G1 = WS_GF + 28 * 8 * 8 * 64 * 8;
constexpr u32 WS_G2 = WS_G1 + 8 * 4 * 8 * 4 * 1024;
constexpr u32 WS_END = WS_G2 + 8 * 32 * 32 * 128;
constexpr u32 WS_STAMP = 8u << 20;

constexpr u32 L_RING = 0;
constexpr u32 L_STG = 131072;
constexpr u32 L_XN = L_STG + 16384;
constexpr u32 XN_STRIDE = 1056;
constexpr u32 L_Q = L_XN + 8 * XN_STRIDE;
constexpr u32 Q_STRIDE = 272;
constexpr u32 L_O = L_Q + 4 * Q_STRIDE;
constexpr u32 L_H = L_O + 8 * Q_STRIDE;
constexpr u32 H_STRIDE = 144;
constexpr u32 L_CNT = L_H + 8 * H_STRIDE;
constexpr u32 L_FLAG = L_CNT + 32;
constexpr u32 L_PF = L_FLAG + 32;
constexpr u32 L_END = L_PF + 256;
constexpr u32 MRG_ML = 4 * 528;
static_assert(L_END <= 163840, "LDS");

constexpr float LOG2E = 1.4426950408889634f;
constexpr float QK_SCALE = 0.08838834764831845f;

__device__ __forceinline__ float bf_lo(u32 w) { return __builtin_bit_cast(float, w << 16); }
__device__ __forceinline__ float bf_hi(u32 w) { return __builtin_bit_cast(float, w & 0xffff0000u); }
__device__ __forceinline__ u32 pack_bf(float a, float b) {
  f32x2 v = {a, b};
  return __builtin_bit_cast(u32, __builtin_convertvector(v, bf16x2));
}
__device__ __forceinline__ float bfr(float a) { return bf_lo(pack_bf(a, a)); }
template <int CTRL> __device__ __forceinline__ float dpp(float x) {
  return __builtin_bit_cast(float, __builtin_amdgcn_mov_dpp(__builtin_bit_cast(int, x), CTRL, 0xf, 0xf, true));
}
constexpr int XOR1 = 0xB1, XOR2 = 0x4E, HMIRROR = 0x141, MIRROR = 0x140, ROR8 = 0x128;
__device__ __forceinline__ float f_of(u32 x) { return __builtin_bit_cast(float, x); }
__device__ __forceinline__ u32 u_of(float x) { return __builtin_bit_cast(u32, x); }
__device__ __forceinline__ float rows_max(float x) {
  auto s = __builtin_amdgcn_permlane16_swap(u_of(x), u_of(x), false, false);
  x = fmaxf(f_of(s[0]), f_of(s[1]));
  auto t = __builtin_amdgcn_permlane32_swap(u_of(x), u_of(x), false, false);
  return fmaxf(f_of(t[0]), f_of(t[1]));
}
__device__ __forceinline__ float rows_sum(float x) {
  auto s = __builtin_amdgcn_permlane16_swap(u_of(x), u_of(x), false, false);
  x = f_of(s[0]) + f_of(s[1]);
  auto t = __builtin_amdgcn_permlane32_swap(u_of(x), u_of(x), false, false);
  return f_of(t[0]) + f_of(t[1]);
}
__device__ __forceinline__ float wave_sum(float x) {
  x += dpp<XOR1>(x); x += dpp<XOR2>(x); x += dpp<HMIRROR>(x); x += dpp<MIRROR>(x);
  return rows_sum(x);
}

template <int W>
__device__ __forceinline__ void dma4(u32 ldsa, const void* sb, u32 v0, u32 v1, u32 v2, u32 v3) {
  u32 keep;
  asm volatile("s_mov_b32 %0, m0\n\ts_mov_b32 m0, %1\n\t"
               "s_waitcnt vmcnt(%7)\n\tglobal_load_lds_dwordx4 %3, %2 nt\n\t"
               "s_waitcnt vmcnt(%7)\n\tglobal_load_lds_dwordx4 %4, %2 offset:1024 nt\n\t"
               "s_waitcnt vmcnt(%7)\n\tglobal_load_lds_dwordx4 %5, %2 offset:2048 nt\n\t"
               "s_waitcnt vmcnt(%7)\n\tglobal_load_lds_dwordx4 %6, %2 offset:3072 nt\n\t"
               "s_mov_b32 m0, %0"
               : "=&s"(keep) : "s"(ldsa), "s"(sb), "v"(v0), "v"(v1), "v"(v2), "v"(v3), "i"(W - 1) : "memory");
}
__device__ __forceinline__ void dma1(u32 ldsa, const void* sb, u32 v0) {
  u32 keep;
  asm volatile("s_mov_b32 %0, m0\n\ts_mov_b32 m0, %1\n\ts_nop 0\n\t"
               "global_load_lds_dwordx4 %3, %2\n\t"
               "s_mov_b32 m0, %0"
               : "=&s"(keep) : "s"(ldsa), "s"(sb), "v"(v0) : "memory");
}
__device__ __forceinline__ void touch(u32 ldsa, const char* p) {
  u32 keep;
  asm volatile("s_mov_b32 %0, m0\n\ts_mov_b32 m0, %1\n\ts_nop 0\n\tglobal_load_lds_dword %2, off\n\ts_mov_b32 m0, %0"
               : "=&s"(keep) : "s"(ldsa), "v"(p) : "memory");
}
#define WAIT_LGKM0() asm volatile("s_waitcnt lgkmcnt(0)" ::: "memory")
#define BARRIER() do { asm volatile("s_waitcnt lgkmcnt(0)" ::: "memory"); __builtin_amdgcn_s_barrier(); asm volatile("" ::: "memory"); } while (0)
template <int N> __device__ __forceinline__ void wait_vm() { asm volatile("s_waitcnt vmcnt(%0)" :: "i"(N) : "memory"); }

__device__ __forceinline__ u32x4 lds_rd128(u32 a) { return *(const LAS u32x4*)a; }
__device__ __forceinline__ u32x2 lds_tr(u32 a) {
  return __builtin_bit_cast(u32x2, __builtin_amdgcn_ds_read_tr16_b64_v4i16((LAS s16x4*)a));
}
__device__ __forceinline__ f32x4 mfma(u32x4 a, u32x4 b, f32x4 c) {
  return __builtin_amdgcn_mfma_f32_16x16x32_bf16(__builtin_bit_cast(bf16x8, a), __builtin_bit_cast(bf16x8, b), c, 0, 0, 0);
}
__device__ __forceinline__ u32x4 stage_frag(u32 a) {
  const u32x2 lo = lds_tr(a), hi = lds_tr(a + 512);
  return u32x4{lo[0], lo[1], hi[0], hi[1]};
}
__device__ __forceinline__ bool gr_ok2(u32x4 v, u32 tag) { return (v[1] == tag) & (v[3] == tag); }
__device__ __forceinline__ bool all_lanes(bool ok) { return __builtin_amdgcn_ballot_w64(!ok) == 0ull; }
template <int SLEEP> __device__ __forceinline__ void wait_cnt4(u32 cnt) {
#pragma unroll 1
  for (u32 spin = 0; spin < SPIN_CAP; ++spin) {
    if (__builtin_amdgcn_readfirstlane(*(volatile LAS u32*)cnt) >= 4u) break;
    if (SLEEP) __builtin_amdgcn_s_sleep(SLEEP);
  }
  asm volatile("" ::: "memory");
}
__device__ __forceinline__ void arrive(u32 cnt, int lane) {
  WAIT_LGKM0();
  if (lane == 0) __hip_atomic_fetch_add((LAS u32*)cnt, 1u, __ATOMIC_RELAXED, __HIP_MEMORY_SCOPE_WORKGROUP);
}
__device__ __forceinline__ void aux_sync(u32 cnt, int lane) { arrive(cnt, lane); wait_cnt4<0>(cnt); }

#define STAMP(i) do { } while (0)
#define STAMP_DECL do { } while (0)
#define STAMP_FLUSH() do { } while (0)

struct StreamState {
  f32x4 acc[8];
  float m, l;
};

template <int W> __device__ __forceinline__ void issue_k(u32 ldsa, const char* src, const u32 (&vk)[4]) {
  dma4<W>(ldsa, src, vk[0], vk[1], vk[2], vk[3]);
  dma4<W>(ldsa + 4096, src + 4096, vk[0], vk[1], vk[2], vk[3]);
}
template <int W> __device__ __forceinline__ void issue_v(u32 ldsa, const char* src, const u32 (&vv)[2]) {
  dma4<W>(ldsa, src, vv[0], vv[1], vv[0], vv[1]);
  dma4<W>(ldsa + 4096, src + 4096, vv[0], vv[1], vv[0], vv[1]);
}

template <int SLOT, int WK, int WV, bool REFILL>
__device__ __forceinline__ void stream_step(StreamState& st, const u32x4 (&qf)[4], const u32 (&ka)[4], const u32 (&va)[8],
                                            u32 ring, const char* kn, const char* vn, const u32 (&vk)[4], const u32 (&vv)[2]) {
  wait_vm<WK>();
  u32x4 kf[2][4];
#pragma unroll
  for (int tt = 0; tt < 2; ++tt)
#pragma unroll
    for (int i = 0; i < 4; ++i) kf[tt][i] = lds_rd128(ka[i] + SLOT * 16384 + tt * 4096);
  WAIT_LGKM0();
  if constexpr (REFILL) issue_k<WIN>(ring + SLOT * 16384, kn, vk);
  f32x4 s0 = {0.f, 0.f, 0.f, 0.f}, s1 = {0.f, 0.f, 0.f, 0.f};
#pragma unroll
  for (int i = 0; i < 4; ++i) { s0 = mfma(kf[0][i], qf[i], s0); s1 = mfma(kf[1][i], qf[i], s1); }
  float t[8];
  {
    const u32 a = pack_bf(s0[0], s0[1]), b = pack_bf(s0[2], s0[3]), c = pack_bf(s1[0], s1[1]), d = pack_bf(s1[2], s1[3]);
    const u32 a2 = pack_bf(bf_lo(a) * QK_SCALE, bf_hi(a) * QK_SCALE), b2 = pack_bf(bf_lo(b) * QK_SCALE, bf_hi(b) * QK_SCALE);
    const u32 c2 = pack_bf(bf_lo(c) * QK_SCALE, bf_hi(c) * QK_SCALE), d2 = pack_bf(bf_lo(d) * QK_SCALE, bf_hi(d) * QK_SCALE);
    t[0] = bf_lo(a2); t[1] = bf_hi(a2); t[2] = bf_lo(b2); t[3] = bf_hi(b2);
    t[4] = bf_lo(c2); t[5] = bf_hi(c2); t[6] = bf_lo(d2); t[7] = bf_hi(d2);
  }
  float mx = fmaxf(fmaxf(fmaxf(t[0], t[1]), fmaxf(t[2], t[3])), fmaxf(fmaxf(t[4], t[5]), fmaxf(t[6], t[7])));
  mx = rows_max(mx);
  if (__builtin_amdgcn_ballot_w64(mx > st.m) != 0ull) {
    const float mn = fmaxf(st.m, mx);
    const float alpha = __builtin_amdgcn_exp2f((st.m - mn) * LOG2E);
    st.m = mn; st.l *= alpha;
#pragma unroll
    for (int c = 0; c < 8; ++c) st.acc[c] *= alpha;
  }
  const float mb = st.m * LOG2E;
  float p[8];
#pragma unroll
  for (int j = 0; j < 8; ++j) p[j] = __builtin_amdgcn_exp2f(fmaf(t[j], LOG2E, -mb));
  st.l += ((p[0] + p[1]) + (p[2] + p[3])) + ((p[4] + p[5]) + (p[6] + p[7]));
  const u32x4 pf = {pack_bf(p[0], p[1]), pack_bf(p[2], p[3]), pack_bf(p[4], p[5]), pack_bf(p[6], p[7])};
  wait_vm<WV>();
  u32x4 vf[8];
#pragma unroll
  for (int c = 0; c < 8; ++c) {
    const u32x2 lo = lds_tr(va[c] + SLOT * 16384 + 8192), hi = lds_tr(va[c] + SLOT * 16384 + 8192 + 4096);
    vf[c] = u32x4{lo[0], lo[1], hi[0], hi[1]};
  }
  WAIT_LGKM0();
  if constexpr (REFILL) issue_v<WIN>(ring + SLOT * 16384 + 8192, vn, vv);
#pragma unroll
  for (int c = 0; c < 8; ++c) st.acc[c] = mfma(vf[c], pf, st.acc[c]);
}

__device__ __forceinline__ void stage_slice(u32 stg, const char* w, u32 row_bytes, int xw, int lane) {
  const u32 vo = (lane >> 3) * row_bytes + (lane & 7) * 16;
#pragma unroll
  for (int j = 0; j < 4; ++j) dma1(stg + j * 1024, w + (size_t)(32 * xw + 8 * j) * row_bytes, vo);
}
__device__ __forceinline__ void project_slice(u32 lds, int row0, int xw, int lane, u32 cnt, __amdgpu_buffer_rsrc_t wsr, u32 dst,
                                              u32 dst_row, u32 tag) {
  const int g = lane >> 4, r = lane & 15;
  const u32 stg = lds + L_STG + xw * 4096;
  const u32x4 a = lds_rd128(lds + L_XN + (r & 7) * XN_STRIDE + (row0 + 32 * xw + 8 * g) * 2);
  const u32 ba = stg + (8 * g + (r >> 2)) * 128 + 8 * (r & 3);
  u32x4 bq[4];
#pragma unroll
  for (int j = 0; j < 4; ++j) bq[j] = stage_frag(ba + 32 * j);
  f32x4 c[4];
#pragma unroll
  for (int j = 0; j < 4; ++j) c[j] = mfma(a, bq[j], f32x4{0.f, 0.f, 0.f, 0.f});
  WAIT_LGKM0();
  if (g < 2) {
#pragma unroll
    for (int j = 0; j < 4; ++j) *(LAS f32x4*)(stg + j * 512 + r * 32 + g * 16) = c[j];
  }
  aux_sync(cnt, lane);
  const int ob = (xw * 64 + lane) >> 5, op = lane & 31;
  const u32 ra = lds + L_STG + (op >> 3) * 512 + ((2 * op) & 15) * 32 + ob * 4;
  float x0[4], x1[4];
#pragma unroll
  for (int w = 0; w < 4; ++w) { x0[w] = *(const LAS float*)(ra + w * 4096); x1[w] = *(const LAS float*)(ra + w * 4096 + 32); }
  const float v0 = (x0[0] + x0[1]) + (x0[2] + x0[3]), v1 = (x1[0] + x1[1]) + (x1[2] + x1[3]);
  __builtin_amdgcn_raw_buffer_store_b128(u32x4{u_of(v0), tag, u_of(v1), tag}, wsr, dst + ob * dst_row + op * 16, 0, 16);
}
template <int N>
__device__ __forceinline__ void poll_issue(u32x4 (&a)[N], __amdgpu_buffer_rsrc_t wsr, const u32 (&off)[N]) {
  asm volatile("" ::: "memory");
#pragma unroll
  for (int k = 0; k < N; ++k) a[k] = __builtin_amdgcn_raw_buffer_load_b128(wsr, off[k], 0, 16);
}
template <int N>
__device__ __forceinline__ bool poll_ok(u32x4 (&a)[N], u32 tag) {
#pragma unroll
  for (int k = 0; k < N; ++k) asm volatile("" : "+v"(a[k]) :: "memory");
  bool ok = true;
#pragma unroll
  for (int k = 0; k < N; ++k) ok = ok & gr_ok2(a[k], tag);
  return all_lanes(ok);
}
template <int N>
__device__ __forceinline__ void gather2(u32x4 (&r)[N], u32x4 (&a)[N], u32x4 (&b)[N], __amdgpu_buffer_rsrc_t wsr, const u32 (&off)[N],
                                        u32 tag) {
#pragma unroll 1
  for (u32 spin = 0; spin < SPIN_CAP; ++spin) {
    poll_issue(b, wsr, off);
    if (poll_ok(a, tag)) {
#pragma unroll
      for (int k = 0; k < N; ++k) r[k] = a[k];
      return;
    }
    poll_issue(a, wsr, off);
    if (poll_ok(b, tag)) {
#pragma unroll
      for (int k = 0; k < N; ++k) r[k] = b[k];
      return;
    }
  }
#pragma unroll
  for (int k = 0; k < N; ++k) r[k] = b[k];
}
template <int N> __device__ __forceinline__ void poll_retire(u32x4 (&a)[N], u32x4 (&b)[N]) {
#pragma unroll
  for (int k = 0; k < N; ++k) asm volatile("" :: "v"(a[k]), "v"(b[k]));
}
template <int N, int SLEEP>
__device__ __forceinline__ void gather1(u32x4 (&v)[N], __amdgpu_buffer_rsrc_t wsr, u32 off, u32 stride, u32 tag) {
#pragma unroll 1
  for (u32 spin = 0; spin < SPIN_CAP; ++spin) {
    asm volatile("" ::: "memory");
#pragma unroll
    for (int k = 0; k < N; ++k) v[k] = __builtin_amdgcn_raw_buffer_load_b128(wsr, off + k * stride, 0, 16);
    bool ok = true;
#pragma unroll
    for (int k = 0; k < N; ++k) ok = ok & gr_ok2(v[k], tag);
    if (all_lanes(ok)) break;
    if (SLEEP) __builtin_amdgcn_s_sleep(SLEEP);
  }
}

}

__global__ void __launch_bounds__(NTHR, 2)
fused_decode_kernel(const u32* __restrict__ x_, const char* __restrict__ kc, const char* __restrict__ vc, const char* __restrict__ wq,
                    const char* __restrict__ wo, const char* __restrict__ wg, const char* __restrict__ wu, const char* __restrict__ wd,
                    u32* __restrict__ out, char* __restrict__ ws) {
  __shared__ __attribute__((aligned(1024))) char smem[L_END];
  u64 pc;
  asm volatile("s_getpc_b64 %0" : "=s"(pc));
  const u32 lds = (u32)(size_t)(LAS char*)smem;
  const int tid = threadIdx.x, lane = tid & 63;
  const int wave = __builtin_amdgcn_readfirstlane(tid >> 6);
  if constexpr (CODE_PF > 0) {
    constexpr int NPF = (CODE_PF + 8191) / 8192;
    if (wave < NPF) {
      u32 keep;
      const u64 base = (pc & ~0xFFull) + (u64)(wave < NPF - 1 ? wave * 8192 : CODE_PF - 8192);
      asm volatile("s_mov_b32 %0, m0\n\ts_mov_b32 m0, %1\n\ts_nop 0\n\tglobal_load_lds_dword %3, %2\n\ts_mov_b32 m0, %0"
                   : "=&s"(keep) : "s"(lds + L_PF), "s"(base), "v"(lane * 128) : "memory");
    }
  }
  const int wg_id = blockIdx.x;
  const int xg = wg_id & 7, rr = wg_id >> 3;
  const int b = xg, kvh = rr >> 2, sp = rr & 3;
  const int g = lane >> 4, r = lane & 15;
  STAMP_DECL;
  STAMP(0);

  if (wave < 4) {
    if (wave == 3 && rr == 31) {
      const char* p = lane < 5    ? wo + (size_t)xg * 524288 + min(lane * 131072, 524284)
                      : lane < 8  ? wd + min(xg * 262144 + (lane - 5) * 131072, 1835004)
                      : lane < 22 ? wg + (lane - 8) * 131072
                      : lane < 36 ? wu + (lane - 22) * 131072
                      : lane < 63 ? ws + min((lane - 36) * 159744, (int)WS_END - 4)
                                  : (const char*)out;
      touch(lds + L_PF, p);
    }
    const size_t blk = (size_t)(b * 8 + kvh) * SK * (DH * 2);
    const char* kb = kc + blk;
    const char* vb = vc + blk;
    const u32 ring = lds + L_RING + wave * 32768;
    const int rot = ((b * 8 + kvh) * ROT) & (NSTEP - 1), v16 = sp * 4 + wave;
    auto tile_off = [&](int s) -> u32 { const int w = (s + rot) & (NSTEP - 1); return w * 131072 + ((v16 ^ (w & 1)) << 13); };
    u32 vk[4], vv[2];
#pragma unroll
    for (int n = 0; n < 4; ++n) vk[n] = g * 256 + (((r ^ g) ^ (4 * n)) << 4);
#pragma unroll
    for (int n = 0; n < 2; ++n) vv[n] = g * 256 + ((r ^ ((4 * n + g) << 1)) << 4);
    if constexpr (ASLEEP0 > 0) {
      asm volatile("" :: "s"(kb), "s"(vb));
      __builtin_amdgcn_s_sleep(ASLEEP0);
    }
    const u32 tstart = (u32)__builtin_amdgcn_s_memrealtime();
    {
      const u32 o0 = tile_off(0), o1 = tile_off(1);
      issue_k<PWIN>(ring, kb + o0, vk); issue_v<PWIN>(ring + 8192, vb + o0, vv);
      issue_k<PWIN>(ring + 16384, kb + o1, vk); issue_v<PWIN>(ring + 16384 + 8192, vb + o1, vv);
    }
    STAMP(1);
    BARRIER();
    STAMP(2);
    u32x4 qf[4];
#pragma unroll
    for (int i = 0; i < 4; ++i) {
      const u32x4 v = lds_rd128(lds + L_Q + (r & 3) * Q_STRIDE + 64 * i + 16 * g);
      qf[i] = r < 4 ? v : u32x4{0u, 0u, 0u, 0u};
    }
    u32 ka[4], va[8];
#pragma unroll
    for (int i = 0; i < 4; ++i) ka[i] = ring + r * 256 + ((((4 * i + g) ^ r)) << 4);
    {
      const int q4 = r >> 2, p4 = r & 3, rho = (4 * g + q4) & 7;
      const u32 base = (4 * g + q4) * 256 + 16 * (p4 >> 1) + 8 * (p4 & 1);
#pragma unroll
      for (int c = 0; c < 8; ++c) va[c] = ring + base + ((c ^ rho) << 5);
    }
    StreamState st;
#pragma unroll
    for (int c = 0; c < 8; ++c) st.acc[c] = f32x4{0.f, 0.f, 0.f, 0.f};
    st.m = -INFINITY; st.l = 0.f;
    auto pace = [&](int s) {
      if constexpr (TAU > 0) { while ((int)((u32)__builtin_amdgcn_s_memrealtime() - (tstart + s * TAU)) < 0) __builtin_amdgcn_s_sleep(2); }
    };
#pragma unroll 1
    for (int t = 0; t < NSTEP - 2; t += 2) {
      const u32 o0 = tile_off(t + 2), o1 = tile_off(t + 3);
      pace(t + 2);
      stream_step<0, 24, 24, true>(st, qf, ka, va, ring, kb + o0, vb + o0, vk, vv);
      pace(t + 3);
      stream_step<1, 24, 24, true>(st, qf, ka, va, ring, kb + o1, vb + o1, vk, vv);
      if (t == FTRIG) arrive(lds + L_CNT + 16, lane);
      if (t == DTRIG) arrive(lds + L_CNT + 20, lane);
    }
    stream_step<0, 24, 16, false>(st, qf, ka, va, ring, kb, vb, vk, vv);
    stream_step<1, 8, 0, false>(st, qf, ka, va, ring, kb, vb, vk, vv);
    STAMP(3);
    const float lsum = rows_sum(st.l);
    if (r < 4) {
#pragma unroll
      for (int c = 0; c < 8; ++c) *(LAS f32x4*)(ring + r * 528 + (16 * c + 4 * g) * 4) = st.acc[c];
      if (g == 0) *(LAS f32x2*)(ring + MRG_ML + r * 8) = f32x2{st.m, lsum};
    }
    BARRIER();
    STAMP(4);
    STAMP_FLUSH();
    return;
  }

  __builtin_amdgcn_s_setprio(3);
  const int xw = wave - 4;
  const u32 stg = lds + L_STG + xw * 4096;
  const u32x4 xa = ((const GAS u32x4*)x_)[(2 * xw) * 64 + lane];
  const u32x4 xb = ((const GAS u32x4*)x_)[(2 * xw + 1) * 64 + lane];
  const int qk = rr >> 3, qc = rr & 7;
  stage_slice(stg, wq + (size_t)(128 * qk) * 8192 + (xg * 512 + qc * 64) * 2, 8192, xw, lane);
  const u32 epoch = __hip_atomic_load((GAS u32*)(ws + WS_EPOCH), __ATOMIC_RELAXED, __HIP_MEMORY_SCOPE_AGENT);
  if (tid < 256 + 8) *(LAS u32*)(lds + L_CNT + (tid - 256) * 4) = 0u;
  const __amdgpu_buffer_rsrc_t wsr = __builtin_amdgcn_make_buffer_rsrc(ws, 0, WS_END, 0x00020000);
#pragma unroll
  for (int rw = 0; rw < 2; ++rw) {
    const u32x4 xv = rw ? xb : xa;
    float v[8] = {bf_lo(xv[0]), bf_hi(xv[0]), bf_lo(xv[1]), bf_hi(xv[1]), bf_lo(xv[2]), bf_hi(xv[2]), bf_lo(xv[3]), bf_hi(xv[3])};
    const float mean = wave_sum(((v[0] + v[1]) + (v[2] + v[3])) + ((v[4] + v[5]) + (v[6] + v[7]))) * (1.0f / DM);
    float ss = 0.f;
#pragma unroll
    for (int j = 0; j < 8; ++j) { const float d = v[j] - mean; ss = fmaf(d, d, ss); }
    const float var = wave_sum(ss) * (1.0f / DM);
    const float mb = bfr(mean), den = bfr(sqrtf(bfr(bfr(var) + 1e-5f)));
    const float rd = 1.0f / den;
    float y[8];
#pragma unroll
    for (int j = 0; j < 8; ++j) {
      const float d = bfr(v[j] - mb), q0 = d * rd;
      y[j] = fmaf(fmaf(-q0, den, d), rd, q0);
    }
    *(LAS u32x4*)(lds + L_XN + (2 * xw + rw) * XN_STRIDE + lane * 16) =
        u32x4{pack_bf(y[0], y[1]), pack_bf(y[2], y[3]), pack_bf(y[4], y[5]), pack_bf(y[6], y[7])};
  }
  STAMP(9);
  const u32 tag = epoch + 1u;
  wait_vm<0>();
  {
    WAIT_LGKM0();
    if (lane == 0) *(volatile LAS u32x2*)(lds + L_FLAG + xw * 8) = u32x2{tag, ~tag};
#pragma unroll 1
    for (u32 spin = 0; spin < SPIN_CAP; ++spin) {
      const u32x4 f0 = *(volatile LAS u32x4*)(lds + L_FLAG), f1 = *(volatile LAS u32x4*)(lds + L_FLAG + 16);
      const bool ok = (f0[0] == tag) & (f0[1] == ~tag) & (f0[2] == tag) & (f0[3] == ~tag) & (f1[0] == tag) & (f1[1] == ~tag) &
                      (f1[2] == tag) & (f1[3] == ~tag);
      if (__builtin_amdgcn_readfirstlane(ok)) break;
    }
    asm volatile("" ::: "memory");
  }
  project_slice(lds, 128 * qk, xw, lane, lds + L_CNT + 0, wsr, WS_GQ + ((xg * 4 + qk) * 8 * 512 + qc * 64) * 8, 512 * 8, tag);
  STAMP(11);
  {
    u32x4 v[4];
    __builtin_amdgcn_s_sleep(QSLEEP0);
    gather1<4, 0>(v, wsr, WS_GQ + ((kvh * 4 * 8 + b) * 512 + xw * 128 + 2 * lane) * 8, 8 * 512 * 8, tag);
    const float q0 = (f_of(v[0][0]) + f_of(v[1][0])) + (f_of(v[2][0]) + f_of(v[3][0]));
    const float q1 = (f_of(v[0][2]) + f_of(v[1][2])) + (f_of(v[2][2]) + f_of(v[3][2]));
    *(LAS u32*)(lds + L_Q + xw * Q_STRIDE + lane * 4) = pack_bf(q0, q1);
  }
  STAMP(2);
  BARRIER();

  const int hh = xg * 4 + (rr >> 3), nj = rr & 7;
  const int fb = rr >> 2, fcb = xg * 4 + (rr & 3);
  const u32 xres = ((const GAS u32*)x_)[fb * 256 + fcb * 8 + (lane & 7)];
  const char* wo_src = wo + (size_t)(hh * 128) * 1024 + nj * 128;
  const char* wd_src = wd + (size_t)(min(hh, FF / 64 - 1) * 64) * 1024 + nj * 128;
  if (xw == 0) touch(lds + L_PF, lane == 0 ? (const char*)out + fb * 1024 : lane == 1 ? ws + WS_G2 + (fb * 32 + fcb) * 4096
                                 : lane == 2 ? ws + WS_G1 + hh * 32 * 1024 : lane == 3 ? ws + WS_G1 + (kvh * 4 * 8 + b) * 4 * 1024
                                 : lane == 4 ? wo_src : wd_src);
  wait_cnt4<16>(lds + L_CNT + 16);
  if (rr < 28) {
    const int ta = xg * 28 + rr, fm = ta & 1, fk = (ta >> 1) & 3, fc = ta >> 3;
    stage_slice(stg, (fm ? wu : wg) + (size_t)(128 * fk) * (FF * 2) + fc * 128, FF * 2, xw, lane);
    wait_vm<0>();
    project_slice(lds, 128 * fk, xw, lane, lds + L_CNT + 4, wsr, WS_GF + (((fc * 2 + fm) * 4 + fk) * 8 * 64) * 8, 64 * 8, tag);
  }
  STAMP(12);
  aux_sync(lds + L_CNT + 8, lane);
  if (hh < FF / 64) {
    const int hb = (xw * 64 + lane) >> 5, hp = lane & 31;
    const u32 ha = WS_GF + ((hh * 8 * 8 + hb) * 64 + 2 * hp) * 8;
    float gu[2][2];
    __builtin_amdgcn_s_sleep(HSLEEP0);
#pragma unroll
    for (int m = 0; m < 2; ++m) {
      u32x4 v[4];
      gather1<4, 32>(v, wsr, ha + m * 4 * (8 * 64 * 8), 8 * 64 * 8, tag);
      gu[m][0] = (f_of(v[0][0]) + f_of(v[1][0])) + (f_of(v[2][0]) + f_of(v[3][0]));
      gu[m][1] = (f_of(v[0][2]) + f_of(v[1][2])) + (f_of(v[2][2]) + f_of(v[3][2]));
    }
    *(LAS u32*)(lds + L_H + hb * H_STRIDE + hp * 4) = pack_bf(bfr(gu[0][0]) * bfr(gu[1][0]), bfr(gu[0][1]) * bfr(gu[1][1]));
  }
  STAMP(13);
  wait_cnt4<16>(lds + L_CNT + 20);
  stage_slice(stg, wo_src, 1024, xw, lane);
  if (hh < FF / 64) {
    const u32 vo = (lane >> 3) * 1024 + (lane & 7) * 16;
#pragma unroll
    for (int j = 0; j < 2; ++j) dma1(lds + L_XN + (16 * xw + 8 * j) * 128, wd_src + (16 * xw + 8 * j) * 1024, vo);
  }
  STAMP(14);
  BARRIER();
  STAMP(4);
  {
    float mw[4], lw[4]; f32x2 ow[4];
#pragma unroll
    for (int w = 0; w < 4; ++w) {
      const f32x2 ml = *(const LAS f32x2*)(lds + L_RING + w * 32768 + MRG_ML + xw * 8);
      mw[w] = ml[0]; lw[w] = ml[1];
      ow[w] = *(const LAS f32x2*)(lds + L_RING + w * 32768 + xw * 528 + lane * 8);
    }
    const float M = fmaxf(fmaxf(mw[0], mw[1]), fmaxf(mw[2], mw[3]));
    float L = 0.f, o0 = 0.f, o1 = 0.f;
#pragma unroll
    for (int w = 0; w < 4; ++w) {
      const float e = __builtin_amdgcn_exp2f((mw[w] - M) * LOG2E);
      L = fmaf(e, lw[w], L); o0 = fmaf(e, ow[w][0], o0); o1 = fmaf(e, ow[w][1], o1);
    }
    const u32 slot = ((kvh * 4 + xw) * 8 + b) * 4 + sp;
    __builtin_amdgcn_raw_buffer_store_b128(u32x4{u_of(o0), tag, u_of(o1), tag}, wsr, WS_G1 + slot * 1024 + lane * 16, 0, 16);
    if (lane == 0) __builtin_amdgcn_raw_buffer_store_b128(u32x4{u_of(M), tag, u_of(L), tag}, wsr, WS_G1M + slot * 16, 0, 16);
  }
  STAMP(8);
  u32 off1[9];
  u32x4 d1[9], d2[9];
  {
    const u32 slot0 = (hh * 8 + 2 * xw) * 4;
#pragma unroll
    for (int s = 0; s < 8; ++s) off1[s] = WS_G1 + (slot0 + s) * 1024 + lane * 16;
    off1[8] = WS_G1M + (slot0 + (lane & 7)) * 16;
    poll_issue(d1, wsr, off1);
  }
  wait_vm<11>();
  BARRIER();
  f32x4 acc = {0.f, 0.f, 0.f, 0.f};
  u32x4 bfo[4];
  {
    const u32 fa = (8 * g + (r >> 2)) * 128 + 32 * xw + 8 * (r & 3);
#pragma unroll
    for (int i = 0; i < 4; ++i) bfo[i] = stage_frag(lds + L_STG + fa + i * 4096);
    if (hh < FF / 64) {
#pragma unroll
      for (int i = 0; i < 2; ++i)
        acc = mfma(lds_rd128(lds + L_H + (r & 7) * H_STRIDE + 64 * i + 16 * g), stage_frag(lds + L_XN + fa + i * 4096), acc);
    }
  }
  {
    u32x4 d[9];
    gather2<9>(d, d1, d2, wsr, off1, tag);
    STAMP(5);
#pragma unroll
    for (int rw = 0; rw < 2; ++rw) {
      float ms[4], ls[4];
#pragma unroll
      for (int s = 0; s < 4; ++s) {
        ms[s] = f_of(__builtin_amdgcn_readlane(d[8][0], 4 * rw + s));
        ls[s] = f_of(__builtin_amdgcn_readlane(d[8][2], 4 * rw + s));
      }
      const float M = fmaxf(fmaxf(ms[0], ms[1]), fmaxf(ms[2], ms[3]));
      float L = 0.f, o0 = 0.f, o1 = 0.f;
#pragma unroll
      for (int s = 0; s < 4; ++s) {
        const float e = __builtin_amdgcn_exp2f((ms[s] - M) * LOG2E);
        L = fmaf(e, ls[s], L); o0 = fmaf(e, f_of(d[4 * rw + s][0]), o0); o1 = fmaf(e, f_of(d[4 * rw + s][2]), o1);
      }
      const float inv = 1.0f / L;
      *(LAS u32*)(lds + L_O + (2 * xw + rw) * Q_STRIDE + lane * 4) = pack_bf(o0 * inv, o1 * inv);
    }
  }
  BARRIER();
#pragma unroll
  for (int i = 0; i < 4; ++i) acc = mfma(lds_rd128(lds + L_O + (r & 7) * Q_STRIDE + 64 * i + 16 * g), bfo[i], acc);
  if (g < 2) {
#pragma unroll
    for (int j = 0; j < 4; ++j)
      __builtin_amdgcn_raw_buffer_store_b64(u32x2{u_of(acc[j]), tag}, wsr,
                                            WS_G2 + (((4 * g + j) * 32 + 4 * nj + xw) * 32 + hh) * 128 + r * 8, 0, 16);
  }
  STAMP(10);
  if (xw != 0) { poll_retire(d1, d2); STAMP_FLUSH(); return; }
  {
    u32 off[4];
#pragma unroll
    for (int m = 0; m < 4; ++m) off[m] = WS_G2 + (fb * 32 + fcb) * 4096 + m * 1024 + lane * 16;
    u32x4 d[4], e1[4], e2[4];
    poll_issue(e1, wsr, off);
    __builtin_amdgcn_s_sleep(PSLEEP);
    gather2<4>(d, e1, e2, wsr, off, tag);
    STAMP(6);
    float v0 = (f_of(d[0][0]) + f_of(d[1][0])) + (f_of(d[2][0]) + f_of(d[3][0]));
    float v1 = (f_of(d[0][2]) + f_of(d[1][2])) + (f_of(d[2][2]) + f_of(d[3][2]));
    v0 += dpp<ROR8>(v0); v1 += dpp<ROR8>(v1);
    v0 = rows_sum(v0); v1 = rows_sum(v1);
    if (lane < 8) __hip_atomic_store((GAS u32*)(out + fb * 256 + fcb * 8 + lane), pack_bf(bf_lo(xres) + v0, bf_hi(xres) + v1),
                                     __ATOMIC_RELAXED, __HIP_MEMORY_SCOPE_AGENT);
    if (wg_id == 0 && lane == 0) __hip_atomic_store((GAS u32*)(ws + WS_EPOCH), tag, __ATOMIC_RELAXED, __HIP_MEMORY_SCOPE_AGENT);
    STAMP(7);
    poll_retire(e1, e2);
    poll_retire(d1, d2);
    STAMP_FLUSH();
  }
}

extern "C" void kernel_launch(void* const* d_in, const int* in_sizes, int n_in, void* d_out, int out_size, void* d_ws,
                              size_t ws_size, hipStream_t stream) {
  (void)in_sizes; (void)n_in; (void)out_size; (void)ws_size;
  hipLaunchKernelGGL(fused_decode_kernel, dim3(NWG), dim3(NTHR), 0, stream, (const u32*)d_in[0], (const char*)d_in[1],
                     (const char*)d_in[2], (const char*)d_in[3], (const char*)d_in[4], (const char*)d_in[6],
                     (const char*)d_in[7], (const char*)d_in[8], (u32*)d_out, (char*)d_ws);
}
```

```cpp
#include <hip/hip_runtime.h>
#include <hip/hip_bf16.h>
#include <stdint.h>
#pragma clang diagnostic ignored "-Wint-to-pointer-cast"

#define GAS __attribute__((address_space(1)))
#define LAS __attribute__((address_space(3)))
typedef unsigned u32;
typedef unsigned long long u64;
typedef u32 u32x2 __attribute__((ext_vector_type(2)));
typedef u32 u32x4 __attribute__((ext_vector_type(4)));
typedef float f32x2 __attribute__((ext_vector_type(2)));
typedef float f32x4 __attribute__((ext_vector_type(4)));
typedef __bf16 bf16x2 __attribute__((ext_vector_type(2)));
typedef __bf16 bf16x8 __attribute__((ext_vector_type(8)));
typedef short s16x4 __attribute__((ext_vector_type(4)));

#define CODE_PF 16128
#define WIN 12
#define PWIN 6
#define TAU 238
#define ASLEEP0 4
#define QSLEEP0 60
#define HSLEEP0 60
#define PSLEEP 12
#define G1P 12
#define FTRIG 8
#define DTRIG 12
#define ROT 7

namespace {

constexpr int DM = 512, SK = 8192, DH = 128, FF = 1792;
constexpr int NWG = 256, NTHR = 512;
constexpr int NSTEP = SK / (32 * 16);
constexpr u32 SPIN_CAP = 1u << 18;

constexpr u32 WS_EPOCH = 0;
constexpr u32 WS_G1M = 4096;
constexpr u32 WS_GQ = 32768;
constexpr u32 WS_GF = WS_GQ + 8 * 4 * 8 * 512 * 8;
constexpr u32 WS_G1 = WS_GF + 28 * 8 * 8 * 64 * 8;
constexpr u32 WS_G2 = WS_G1 + 8 * 4 * 8 * 4 * 1024;
constexpr u32 WS_END = WS_G2 + 8 * 32 * 32 * 128;
constexpr u32 WS_STAMP = 8u << 20;

constexpr u32 L_RING = 0;
constexpr u32 L_STG = 131072;
constexpr u32 L_XN = L_STG + 16384;
constexpr u32 XN_STRIDE = 1056;
constexpr u32 L_Q = L_XN + 8 * XN_STRIDE;
constexpr u32 Q_STRIDE = 272;
constexpr u32 L_O = L_Q + 4 * Q_STRIDE;
constexpr u32 L_H = L_O + 8 * Q_STRIDE;
constexpr u32 H_STRIDE = 144;
constexpr u32 L_CNT = L_H + 8 * H_STRIDE;
constexpr u32 L_FLAG = L_CNT + 32;
constexpr u32 L_PF = L_FLAG + 32;
constexpr u32 L_END = L_PF + 256;
constexpr u32 MRG_ML = 4 * 528;
static_assert(L_END <= 163840, "LDS");

constexpr float LOG2E = 1.4426950408889634f;
constexpr float QK_SCALE = 0.08838834764831845f;

__device__ __forceinline__ float bf_lo(u32 w) { return __builtin_bit_cast(float, w << 16); }
__device__ __forceinline__ float bf_hi(u32 w) { return __builtin_bit_cast(float, w & 0xffff0000u); }
__device__ __forceinline__ u32 pack_bf(float a, float b) {
  f32x2 v = {a, b};
  return __builtin_bit_cast(u32, __builtin_convertvector(v, bf16x2));
}
__device__ __forceinline__ float bfr(float a) { return bf_lo(pack_bf(a, a)); }
template <int CTRL> __device__ __forceinline__ float dpp(float x) {
  return __builtin_bit_cast(float, __builtin_amdgcn_mov_dpp(__builtin_bit_cast(int, x), CTRL, 0xf, 0xf, true));
}
constexpr int XOR1 = 0xB1, XOR2 = 0x4E, HMIRROR = 0x141, MIRROR = 0x140, ROR8 = 0x128;
__device__ __forceinline__ float f_of(u32 x) { return __builtin_bit_cast(float, x); }
__device__ __forceinline__ u32 u_of(float x) { return __builtin_bit_cast(u32, x); }
__device__ __forceinline__ float rows_max(float x) {
  auto s = __builtin_amdgcn_permlane16_swap(u_of(x), u_of(x), false, false);
  x = fmaxf(f_of(s[0]), f_of(s[1]));
  auto t = __builtin_amdgcn_permlane32_swap(u_of(x), u_of(x), false, false);
  return fmaxf(f_of(t[0]), f_of(t[1]));
}
__device__ __forceinline__ float rows_sum(float x) {
  auto s = __builtin_amdgcn_permlane16_swap(u_of(x), u_of(x), false, false);
  x = f_of(s[0]) + f_of(s[1]);
  auto t = __builtin_amdgcn_permlane32_swap(u_of(x), u_of(x), false, false);
  return f_of(t[0]) + f_of(t[1]);
}
__device__ __forceinline__ float wave_sum(float x) {
  x += dpp<XOR1>(x); x += dpp<XOR2>(x); x += dpp<HMIRROR>(x); x += dpp<MIRROR>(x);
  return rows_sum(x);
}

template <int W>
__device__ __forceinline__ void dma4(u32 ldsa, const void* sb, u32 v0, u32 v1, u32 v2, u32 v3) {
  u32 keep;
  asm volatile("s_mov_b32 %0, m0\n\ts_mov_b32 m0, %1\n\t"
               "s_waitcnt vmcnt(%7)\n\tglobal_load_lds_dwordx4 %3, %2 nt\n\t"
               "s_waitcnt vmcnt(%7)\n\tglobal_load_lds_dwordx4 %4, %2 offset:1024 nt\n\t"
               "s_waitcnt vmcnt(%7)\n\tglobal_load_lds_dwordx4 %5, %2 offset:2048 nt\n\t"
               "s_waitcnt vmcnt(%7)\n\tglobal_load_lds_dwordx4 %6, %2 offset:3072 nt\n\t"
               "s_mov_b32 m0, %0"
               : "=&s"(keep) : "s"(ldsa), "s"(sb), "v"(v0), "v"(v1), "v"(v2), "v"(v3), "i"(W - 1) : "memory");
}
__device__ __forceinline__ void dma1(u32 ldsa, const void* sb, u32 v0) {
  u32 keep;
  asm volatile("s_mov_b32 %0, m0\n\ts_mov_b32 m0, %1\n\ts_nop 0\n\t"
               "global_load_lds_dwordx4 %3, %2\n\t"
               "s_mov_b32 m0, %0"
               : "=&s"(keep) : "s"(ldsa), "s"(sb), "v"(v0) : "memory");
}
__device__ __forceinline__ void touch(u32 ldsa, const char* p) {
  u32 keep;
  asm volatile("s_mov_b32 %0, m0\n\ts_mov_b32 m0, %1\n\ts_nop 0\n\tglobal_load_lds_dword %2, off\n\ts_mov_b32 m0, %0"
               : "=&s"(keep) : "s"(ldsa), "v"(p) : "memory");
}
#define WAIT_LGKM0() asm volatile("s_waitcnt lgkmcnt(0)" ::: "memory")
#define BARRIER() do { asm volatile("s_waitcnt lgkmcnt(0)" ::: "memory"); __builtin_amdgcn_s_barrier(); asm volatile("" ::: "memory"); } while (0)
template <int N> __device__ __forceinline__ void wait_vm() { asm volatile("s_waitcnt vmcnt(%0)" :: "i"(N) : "memory"); }

__device__ __forceinline__ u32x4 lds_rd128(u32 a) { return *(const LAS u32x4*)a; }
__device__ __forceinline__ u32x2 lds_tr(u32 a) {
  return __builtin_bit_cast(u32x2, __builtin_amdgcn_ds_read_tr16_b64_v4i16((LAS s16x4*)a));
}
__device__ __forceinline__ f32x4 mfma(u32x4 a, u32x4 b, f32x4 c) {
  return __builtin_amdgcn_mfma_f32_16x16x32_bf16(__builtin_bit_cast(bf16x8, a), __builtin_bit_cast(bf16x8, b), c, 0, 0, 0);
}
__device__ __forceinline__ u32x4 stage_frag(u32 a) {
  const u32x2 lo = lds_tr(a), hi = lds_tr(a + 512);
  return u32x4{lo[0], lo[1], hi[0], hi[1]};
}
__device__ __forceinline__ bool gr_ok2(u32x4 v, u32 tag) { return (v[1] == tag) & (v[3] == tag); }
__device__ __forceinline__ bool all_lanes(bool ok) { return __builtin_amdgcn_ballot_w64(!ok) == 0ull; }
template <int SLEEP> __device__ __forceinline__ void wait_cnt4(u32 cnt) {
#pragma unroll 1
  for (u32 spin = 0; spin < SPIN_CAP; ++spin) {
    if (__builtin_amdgcn_readfirstlane(*(volatile LAS u32*)cnt) >= 4u) break;
    if (SLEEP) __builtin_amdgcn_s_sleep(SLEEP);
  }
  asm volatile("" ::: "memory");
}
__device__ __forceinline__ void arrive(u32 cnt, int lane) {
  WAIT_LGKM0();
  if (lane == 0) __hip_atomic_fetch_add((LAS u32*)cnt, 1u, __ATOMIC_RELAXED, __HIP_MEMORY_SCOPE_WORKGROUP);
}
__device__ __forceinline__ void aux_sync(u32 cnt, int lane) { arrive(cnt, lane); wait_cnt4<0>(cnt); }

#define STAMP(i) do { } while (0)
#define STAMP_DECL do { } while (0)
#define STAMP_FLUSH() do { } while (0)

struct StreamState {
  f32x4 acc[8];
  float m, l;
};

template <int W> __device__ __forceinline__ void issue_k(u32 ldsa, const char* src, const u32 (&vk)[4]) {
  dma4<W>(ldsa, src, vk[0], vk[1], vk[2], vk[3]);
  dma4<W>(ldsa + 4096, src + 4096, vk[0], vk[1], vk[2], vk[3]);
}
template <int W> __device__ __forceinline__ void issue_v(u32 ldsa, const char* src, const u32 (&vv)[2]) {
  dma4<W>(ldsa, src, vv[0], vv[1], vv[0], vv[1]);
  dma4<W>(ldsa + 4096, src + 4096, vv[0], vv[1], vv[0], vv[1]);
}

template <int SLOT, int WK, int WV, bool REFILL>
__device__ __forceinline__ void stream_step(StreamState& st, const u32x4 (&qf)[4], const u32 (&ka)[4], const u32 (&va)[8],
                                            u32 ring, const char* kn, const char* vn, const u32 (&vk)[4], const u32 (&vv)[2]) {
  wait_vm<WK>();
  u32x4 kf[2][4];
#pragma unroll
  for (int tt = 0; tt < 2; ++tt)
#pragma unroll
    for (int i = 0; i < 4; ++i) kf[tt][i] = lds_rd128(ka[i] + SLOT * 16384 + tt * 4096);
  WAIT_LGKM0();
  if constexpr (REFILL) issue_k<WIN>(ring + SLOT * 16384, kn, vk);
  f32x4 s0 = {0.f, 0.f, 0.f, 0.f}, s1 = {0.f, 0.f, 0.f, 0.f};
#pragma unroll
  for (int i = 0; i < 4; ++i) { s0 = mfma(kf[0][i], qf[i], s0); s1 = mfma(kf[1][i], qf[i], s1); }
  float t[8];
  {
    const u32 a = pack_bf(s0[0], s0[1]), b = pack_bf(s0[2], s0[3]), c = pack_bf(s1[0], s1[1]), d = pack_bf(s1[2], s1[3]);
    const u32 a2 = pack_bf(bf_lo(a) * QK_SCALE, bf_hi(a) * QK_SCALE), b2 = pack_bf(bf_lo(b) * QK_SCALE, bf_hi(b) * QK_SCALE);
    const u32 c2 = pack_bf(bf_lo(c) * QK_SCALE, bf_hi(c) * QK_SCALE), d2 = pack_bf(bf_lo(d) * QK_SCALE, bf_hi(d) * QK_SCALE);
    t[0] = bf_lo(a2); t[1] = bf_hi(a2); t[2] = bf_lo(b2); t[3] = bf_hi(b2);
    t[4] = bf_lo(c2); t[5] = bf_hi(c2); t[6] = bf_lo(d2); t[7] = bf_hi(d2);
  }
  float mx = fmaxf(fmaxf(fmaxf(t[0], t[1]), fmaxf(t[2], t[3])), fmaxf(fmaxf(t[4], t[5]), fmaxf(t[6], t[7])));
  mx = rows_max(mx);
  if (__builtin_amdgcn_ballot_w64(mx > st.m) != 0ull) {
    const float mn = fmaxf(st.m, mx);
    const float alpha = __builtin_amdgcn_exp2f((st.m - mn) * LOG2E);
    st.m = mn; st.l *= alpha;
#pragma unroll
    for (int c = 0; c < 8; ++c) st.acc[c] *= alpha;
  }
  const float mb = st.m * LOG2E;
  float p[8];
#pragma unroll
  for (int j = 0; j < 8; ++j) p[j] = __builtin_amdgcn_exp2f(fmaf(t[j], LOG2E, -mb));
  st.l += ((p[0] + p[1]) + (p[2] + p[3])) + ((p[4] + p[5]) + (p[6] + p[7]));
  const u32x4 pf = {pack_bf(p[0], p[1]), pack_bf(p[2], p[3]), pack_bf(p[4], p[5]), pack_bf(p[6], p[7])};
  wait_vm<WV>();
  u32x4 vf[8];
#pragma unroll
  for (int c = 0; c < 8; ++c) {
    const u32x2 lo = lds_tr(va[c] + SLOT * 16384 + 8192), hi = lds_tr(va[c] + SLOT * 16384 + 8192 + 4096);
    vf[c] = u32x4{lo[0], lo[1], hi[0], hi[1]};
  }
  WAIT_LGKM0();
  if constexpr (REFILL) issue_v<WIN>(ring + SLOT * 16384 + 8192, vn, vv);
#pragma unroll
  for (int c = 0; c < 8; ++c) st.acc[c] = mfma(vf[c], pf, st.acc[c]);
}

__device__ __forceinline__ void stage_slice(u32 stg, const char* w, u32 row_bytes, int xw, int lane) {
  const u32 vo = (lane >> 3) * row_bytes + (lane & 7) * 16;
#pragma unroll
  for (int j = 0; j < 4; ++j) dma1(stg + j * 1024, w + (size_t)(32 * xw + 8 * j) * row_bytes, vo);
}
__device__ __forceinline__ void project_slice(u32 lds, int row0, int xw, int lane, u32 cnt, __amdgpu_buffer_rsrc_t wsr, u32 dst,
                                              u32 dst_row, u32 tag) {
  const int g = lane >> 4, r = lane & 15;
  const u32 stg = lds + L_STG + xw * 4096;
  const u32x4 a = lds_rd128(lds + L_XN + (r & 7) * XN_STRIDE + (row0 + 32 * xw + 8 * g) * 2);
  const u32 ba = stg + (8 * g + (r >> 2)) * 128 + 8 * (r & 3);
  u32x4 bq[4];
#pragma unroll
  for (int j = 0; j < 4; ++j) bq[j] = stage_frag(ba + 32 * j);
  f32x4 c[4];
#pragma unroll
  for (int j = 0; j < 4; ++j) c[j] = mfma(a, bq[j], f32x4{0.f, 0.f, 0.f, 0.f});
  WAIT_LGKM0();
  if (g < 2) {
#pragma unroll
    for (int j = 0; j < 4; ++j) *(LAS f32x4*)(stg + j * 512 + r * 32 + g * 16) = c[j];
  }
  aux_sync(cnt, lane);
  const int ob = (xw * 64 + lane) >> 5, op = lane & 31;
  const u32 ra = lds + L_STG + (op >> 3) * 512 + ((2 * op) & 15) * 32 + ob * 4;
  float x0[4], x1[4];
#pragma unroll
  for (int w = 0; w < 4; ++w) { x0[w] = *(const LAS float*)(ra + w * 4096); x1[w] = *(const LAS float*)(ra + w * 4096 + 32); }
  const float v0 = (x0[0] + x0[1]) + (x0[2] + x0[3]), v1 = (x1[0] + x1[1]) + (x1[2] + x1[3]);
  __builtin_amdgcn_raw_buffer_store_b128(u32x4{u_of(v0), tag, u_of(v1), tag}, wsr, dst + ob * dst_row + op * 16, 0, 16);
}
template <int N>
__device__ __forceinline__ void poll_issue(u32x4 (&a)[N], __amdgpu_buffer_rsrc_t wsr, const u32 (&off)[N]) {
  asm volatile("" ::: "memory");
#pragma unroll
  for (int k = 0; k < N; ++k) a[k] = __builtin_amdgcn_raw_buffer_load_b128(wsr, off[k], 0, 16);
}
template <int N>
__device__ __forceinline__ bool poll_ok(u32x4 (&a)[N], u32 tag) {
#pragma unroll
  for (int k = 0; k < N; ++k) asm volatile("" : "+v"(a[k]) :: "memory");
  bool ok = true;
#pragma unroll
  for (int k = 0; k < N; ++k) ok = ok & gr_ok2(a[k], tag);
  return all_lanes(ok);
}
template <int N>
__device__ __forceinline__ void gather2(u32x4 (&r)[N], u32x4 (&a)[N], u32x4 (&b)[N], __amdgpu_buffer_rsrc_t wsr, const u32 (&off)[N],
                                        u32 tag) {
#pragma unroll 1
  for (u32 spin = 0; spin < SPIN_CAP; ++spin) {
    poll_issue(b, wsr, off);
    if (poll_ok(a, tag)) {
#pragma unroll
      for (int k = 0; k < N; ++k) r[k] = a[k];
      return;
    }
    poll_issue(a, wsr, off);
    if (poll_ok(b, tag)) {
#pragma unroll
      for (int k = 0; k < N; ++k) r[k] = b[k];
      return;
    }
  }
#pragma unroll
  for (int k = 0; k < N; ++k) r[k] = b[k];
}
template <int N> __device__ __forceinline__ void poll_retire(u32x4 (&a)[N], u32x4 (&b)[N]) {
#pragma unroll
  for (int k = 0; k < N; ++k) asm volatile("" :: "v"(a[k]), "v"(b[k]));
}
template <int N, int SLEEP>
__device__ __forceinline__ void gather1(u32x4 (&v)[N], __amdgpu_buffer_rsrc_t wsr, u32 off, u32 stride, u32 tag) {
#pragma unroll 1
  for (u32 spin = 0; spin < SPIN_CAP; ++spin) {
    asm volatile("" ::: "memory");
#pragma unroll
    for (int k = 0; k < N; ++k) v[k] = __builtin_amdgcn_raw_buffer_load_b128(wsr, off + k * stride, 0, 16);
    bool ok = true;
#pragma unroll
    for (int k = 0; k < N; ++k) ok = ok & gr_ok2(v[k], tag);
    if (all_lanes(ok)) break;
    if (SLEEP) __builtin_amdgcn_s_sleep(SLEEP);
  }
}

}

__global__ void __launch_bounds__(NTHR, 2)
fused_decode_kernel(const u32* __restrict__ x_, const char* __restrict__ kc, const char* __restrict__ vc, const char* __restrict__ wq,
                    const char* __restrict__ wo, const char* __restrict__ wg, const char* __restrict__ wu, const char* __restrict__ wd,
                    u32* __restrict__ out, char* __restrict__ ws) {
  __shared__ __attribute__((aligned(1024))) char smem[L_END];
  u64 pc;
  asm volatile("s_getpc_b64 %0" : "=s"(pc));
  const u32 lds = (u32)(size_t)(LAS char*)smem;
  const int tid = threadIdx.x, lane = tid & 63;
  const int wave = __builtin_amdgcn_readfirstlane(tid >> 6);
  if constexpr (CODE_PF > 0) {
    constexpr int NPF = (CODE_PF + 8191) / 8192;
    if (wave < NPF) {
      u32 keep;
      const u64 base = (pc & ~0xFFull) + (u64)(wave < NPF - 1 ? wave * 8192 : CODE_PF - 8192);
      asm volatile("s_mov_b32 %0, m0\n\ts_mov_b32 m0, %1\n\ts_nop 0\n\tglobal_load_lds_dword %3, %2\n\ts_mov_b32 m0, %0"
                   : "=&s"(keep) : "s"(lds + L_PF), "s"(base), "v"(lane * 128) : "memory");
    }
  }
  const int wg_id = blockIdx.x;
  const int xg = wg_id & 7, rr = wg_id >> 3;
  const int b = xg, kvh = rr >> 2, sp = rr & 3;
  const int g = lane >> 4, r = lane & 15;
  STAMP_DECL;
  STAMP(0);

  if (wave < 4) {
    if (wave == 3 && rr == 31) {
      const char* p = lane < 5    ? wo + (size_t)xg * 524288 + min(lane * 131072, 524284)
                      : lane < 8  ? wd + min(xg * 262144 + (lane - 5) * 131072, 1835004)
                      : lane < 22 ? wg + (lane - 8) * 131072
                      : lane < 36 ? wu + (lane - 22) * 131072
                      : lane < 63 ? ws + min((lane - 36) * 159744, (int)WS_END - 4)
                                  : (const char*)out;
      touch(lds + L_PF, p);
    }
    const size_t blk = (size_t)(b * 8 + kvh) * SK * (DH * 2);
    const char* kb = kc + blk;
    const char* vb = vc + blk;
    const u32 ring = lds + L_RING + wave * 32768;
    const int rot = ((b * 8 + kvh) * ROT) & (NSTEP - 1), v16 = sp * 4 + wave;
    auto tile_off = [&](int s) -> u32 { const int w = (s + rot) & (NSTEP - 1); return w * 131072 + ((v16 ^ (w & 1)) << 13); };
    u32 vk[4], vv[2];
#pragma unroll
    for (int n = 0; n < 4; ++n) vk[n] = g * 256 + (((r ^ g) ^ (4 * n)) << 4);
#pragma unroll
    for (int n = 0; n < 2; ++n) vv[n] = g * 256 + ((r ^ ((4 * n + g) << 1)) << 4);
    if constexpr (ASLEEP0 > 0) {
      asm volatile("" :: "s"(kb), "s"(vb));
      __builtin_amdgcn_s_sleep(ASLEEP0);
    }
    const u32 tstart = (u32)__builtin_amdgcn_s_memrealtime();
    {
      const u32 o0 = tile_off(0), o1 = tile_off(1);
      issue_k<PWIN>(ring, kb + o0, vk); issue_v<PWIN>(ring + 8192, vb + o0, vv);
      issue_k<PWIN>(ring + 16384, kb + o1, vk); issue_v<PWIN>(ring + 16384 + 8192, vb + o1, vv);
    }
    STAMP(1);
    BARRIER();
    STAMP(2);
    u32x4 qf[4];
#pragma unroll
    for (int i = 0; i < 4; ++i) {
      const u32x4 v = lds_rd128(lds + L_Q + (r & 3) * Q_STRIDE + 64 * i + 16 * g);
      qf[i] = r < 4 ? v : u32x4{0u, 0u, 0u, 0u};
    }
    u32 ka[4], va[8];
#pragma unroll
    for (int i = 0; i < 4; ++i) ka[i] = ring + r * 256 + ((((4 * i + g) ^ r)) << 4);
    {
      const int q4 = r >> 2, p4 = r & 3, rho = (4 * g + q4) & 7;
      const u32 base = (4 * g + q4) * 256 + 16 * (p4 >> 1) + 8 * (p4 & 1);
#pragma unroll
      for (int c = 0; c < 8; ++c) va[c] = ring + base + ((c ^ rho) << 5);
    }
    StreamState st;
#pragma unroll
    for (int c = 0; c < 8; ++c) st.acc[c] = f32x4{0.f, 0.f, 0.f, 0.f};
    st.m = -INFINITY; st.l = 0.f;
    const u32 tnow = (u32)__builtin_amdgcn_s_memrealtime();
    const int left = (int)(tstart + (NSTEP - 1) * TAU - tnow);
    const u32 tau = left > 0 ? ((u32)left * 5041u) >> 16 : 0u, torg = tnow - 2 * tau;
    auto pace = [&](int s) {
      if constexpr (TAU > 0) { while ((int)((u32)__builtin_amdgcn_s_memrealtime() - (torg + s * tau)) < 0) __builtin_amdgcn_s_sleep(2); }
    };
#pragma unroll 1
    for (int t = 0; t < NSTEP - 2; t += 2) {
      const u32 o0 = tile_off(t + 2), o1 = tile_off(t + 3);
      pace(t + 2);
      stream_step<0, 24, 24, true>(st, qf, ka, va, ring, kb + o0, vb + o0, vk, vv);
      pace(t + 3);
      stream_step<1, 24, 24, true>(st, qf, ka, va, ring, kb + o1, vb + o1, vk, vv);
      if (t == FTRIG) arrive(lds + L_CNT + 16, lane);
      if (t == DTRIG) arrive(lds + L_CNT + 20, lane);
    }
    stream_step<0, 24, 16, false>(st, qf, ka, va, ring, kb, vb, vk, vv);
    stream_step<1, 8, 0, false>(st, qf, ka, va, ring, kb, vb, vk, vv);
    STAMP(3);
    const float lsum = rows_sum(st.l);
    if (r < 4) {
#pragma unroll
      for (int c = 0; c < 8; ++c) *(LAS f32x4*)(ring + r * 528 + (16 * c + 4 * g) * 4) = st.acc[c];
      if (g == 0) *(LAS f32x2*)(ring + MRG_ML + r * 8) = f32x2{st.m, lsum};
    }
    BARRIER();
    STAMP(4);
    STAMP_FLUSH();
    return;
  }

  __builtin_amdgcn_s_setprio(3);
  const int xw = wave - 4;
  const u32 stg = lds + L_STG + xw * 4096;
  const u32x4 xa = ((const GAS u32x4*)x_)[(2 * xw) * 64 + lane];
  const u32x4 xb = ((const GAS u32x4*)x_)[(2 * xw + 1) * 64 + lane];
  const int qk = rr >> 3, qc = rr & 7;
  stage_slice(stg, wq + (size_t)(128 * qk) * 8192 + (xg * 512 + qc * 64) * 2, 8192, xw, lane);
  const u32 epoch = __hip_atomic_load((GAS u32*)(ws + WS_EPOCH), __ATOMIC_RELAXED, __HIP_MEMORY_SCOPE_AGENT);
  if (tid < 256 + 8) *(LAS u32*)(lds + L_CNT + (tid - 256) * 4) = 0u;
  const __amdgpu_buffer_rsrc_t wsr = __builtin_amdgcn_make_buffer_rsrc(ws, 0, WS_END, 0x00020000);
#pragma unroll
  for (int rw = 0; rw < 2; ++rw) {
    const u32x4 xv = rw ? xb : xa;
    float v[8] = {bf_lo(xv[0]), bf_hi(xv[0]), bf_lo(xv[1]), bf_hi(xv[1]), bf_lo(xv[2]), bf_hi(xv[2]), bf_lo(xv[3]), bf_hi(xv[3])};
    const float mean = wave_sum(((v[0] + v[1]) + (v[2] + v[3])) + ((v[4] + v[5]) + (v[6] + v[7]))) * (1.0f / DM);
    float ss = 0.f;
#pragma unroll
    for (int j = 0; j < 8; ++j) { const float d = v[j] - mean; ss = fmaf(d, d, ss); }
    const float var = wave_sum(ss) * (1.0f / DM);
    const float mb = bfr(mean), den = bfr(sqrtf(bfr(bfr(var) + 1e-5f)));
    const float rd = 1.0f / den;
    float y[8];
#pragma unroll
    for (int j = 0; j < 8; ++j) {
      const float d = bfr(v[j] - mb), q0 = d * rd;
      y[j] = fmaf(fmaf(-q0, den, d), rd, q0);
    }
    *(LAS u32x4*)(lds + L_XN + (2 * xw + rw) * XN_STRIDE + lane * 16) =
        u32x4{pack_bf(y[0], y[1]), pack_bf(y[2], y[3]), pack_bf(y[4], y[5]), pack_bf(y[6], y[7])};
  }
  STAMP(9);
  const u32 tag = epoch + 1u;
  wait_vm<0>();
  {
    WAIT_LGKM0();
    if (lane == 0) *(volatile LAS u32x2*)(lds + L_FLAG + xw * 8) = u32x2{tag, ~tag};
#pragma unroll 1
    for (u32 spin = 0; spin < SPIN_CAP; ++spin) {
      const u32x4 f0 = *(volatile LAS u32x4*)(lds + L_FLAG), f1 = *(volatile LAS u32x4*)(lds + L_FLAG + 16);
      const bool ok = (f0[0] == tag) & (f0[1] == ~tag) & (f0[2] == tag) & (f0[3] == ~tag) & (f1[0] == tag) & (f1[1] == ~tag) &
                      (f1[2] == tag) & (f1[3] == ~tag);
      if (__builtin_amdgcn_readfirstlane(ok)) break;
    }
    asm volatile("" ::: "memory");
  }
  project_slice(lds, 128 * qk, xw, lane, lds + L_CNT + 0, wsr, WS_GQ + ((xg * 4 + qk) * 8 * 512 + qc * 64) * 8, 512 * 8, tag);
  STAMP(11);
  {
    u32x4 v[4];
    __builtin_amdgcn_s_sleep(QSLEEP0);
    gather1<4, 0>(v, wsr, WS_GQ + ((kvh * 4 * 8 + b) * 512 + xw * 128 + 2 * lane) * 8, 8 * 512 * 8, tag);
    const float q0 = (f_of(v[0][0]) + f_of(v[1][0])) + (f_of(v[2][0]) + f_of(v[3][0]));
    const float q1 = (f_of(v[0][2]) + f_of(v[1][2])) + (f_of(v[2][2]) + f_of(v[3][2]));
    *(LAS u32*)(lds + L_Q + xw * Q_STRIDE + lane * 4) = pack_bf(q0, q1);
  }
  STAMP(2);
  BARRIER();

  const int hh = xg * 4 + (rr >> 3), nj = rr & 7;
  const int fb = rr >> 2, fcb = xg * 4 + (rr & 3);
  const u32 xres = ((const GAS u32*)x_)[fb * 256 + fcb * 8 + (lane & 7)];
  const char* wo_src = wo + (size_t)(hh * 128) * 1024 + nj * 128;
  const char* wd_src = wd + (size_t)(min(hh, FF / 64 - 1) * 64) * 1024 + nj * 128;
  if (xw == 0) touch(lds + L_PF, lane == 0 ? (const char*)out + fb * 1024 : lane == 1 ? ws + WS_G2 + (fb * 32 + fcb) * 4096
                                 : lane == 2 ? ws + WS_G1 + hh * 32 * 1024 : lane == 3 ? ws + WS_G1 + (kvh * 4 * 8 + b) * 4 * 1024
                                 : lane == 4 ? wo_src : wd_src);
  wait_cnt4<16>(lds + L_CNT + 16);
  if (rr < 28) {
    const int ta = xg * 28 + rr, fm = ta & 1, fk = (ta >> 1) & 3, fc = ta >> 3;
    stage_slice(stg, (fm ? wu : wg) + (size_t)(128 * fk) * (FF * 2) + fc * 128, FF * 2, xw, lane);
    wait_vm<0>();
    project_slice(lds, 128 * fk, xw, lane, lds + L_CNT + 4, wsr, WS_GF + (((fc * 2 + fm) * 4 + fk) * 8 * 64) * 8, 64 * 8, tag);
  }
  STAMP(12);
  aux_sync(lds + L_CNT + 8, lane);
  if (hh < FF / 64) {
    const int hb = (xw * 64 + lane) >> 5, hp = lane & 31;
    const u32 ha = WS_GF + ((hh * 8 * 8 + hb) * 64 + 2 * hp) * 8;
    float gu[2][2];
    __builtin_amdgcn_s_sleep(HSLEEP0);
#pragma unroll
    for (int m = 0; m < 2; ++m) {
      u32x4 v[4];
      gather1<4, 32>(v, wsr, ha + m * 4 * (8 * 64 * 8), 8 * 64 * 8, tag);
      gu[m][0] = (f_of(v[0][0]) + f_of(v[1][0])) + (f_of(v[2][0]) + f_of(v[3][0]));
      gu[m][1] = (f_of(v[0][2]) + f_of(v[1][2])) + (f_of(v[2][2]) + f_of(v[3][2]));
    }
    *(LAS u32*)(lds + L_H + hb * H_STRIDE + hp * 4) = pack_bf(bfr(gu[0][0]) * bfr(gu[1][0]), bfr(gu[0][1]) * bfr(gu[1][1]));
  }
  STAMP(13);
  wait_cnt4<16>(lds + L_CNT + 20);
  stage_slice(stg, wo_src, 1024, xw, lane);
  if (hh < FF / 64) {
    const u32 vo = (lane >> 3) * 1024 + (lane & 7) * 16;
#pragma unroll
    for (int j = 0; j < 2; ++j) dma1(lds + L_XN + (16 * xw + 8 * j) * 128, wd_src + (16 * xw + 8 * j) * 1024, vo);
  }
  STAMP(14);
  BARRIER();
  STAMP(4);
  {
    float mw[4], lw[4]; f32x2 ow[4];
#pragma unroll
    for (int w = 0; w < 4; ++w) {
      const f32x2 ml = *(const LAS f32x2*)(lds + L_RING + w * 32768 + MRG_ML + xw * 8);
      mw[w] = ml[0]; lw[w] = ml[1];
      ow[w] = *(const LAS f32x2*)(lds + L_RING + w * 32768 + xw * 528 + lane * 8);
    }
    const float M = fmaxf(fmaxf(mw[0], mw[1]), fmaxf(mw[2], mw[3]));
    float L = 0.f, o0 = 0.f, o1 = 0.f;
#pragma unroll
    for (int w = 0; w < 4; ++w) {
      const float e = __builtin_amdgcn_exp2f((mw[w] - M) * LOG2E);
      L = fmaf(e, lw[w], L); o0 = fmaf(e, ow[w][0], o0); o1 = fmaf(e, ow[w][1], o1);
    }
    const u32 slot = ((kvh * 4 + xw) * 8 + b) * 4 + sp;
    __builtin_amdgcn_raw_buffer_store_b128(u32x4{u_of(o0), tag, u_of(o1), tag}, wsr, WS_G1 + slot * 1024 + lane * 16, 0, 16);
    if (lane == 0) __builtin_amdgcn_raw_buffer_store_b128(u32x4{u_of(M), tag, u_of(L), tag}, wsr, WS_G1M + slot * 16, 0, 16);
  }
  STAMP(8);
  u32 off1[9];
  u32x4 d1[9], d2[9];
  {
    const u32 slot0 = (hh * 8 + 2 * xw) * 4;
#pragma unroll
    for (int s = 0; s < 8; ++s) off1[s] = WS_G1 + (slot0 + s) * 1024 + lane * 16;
    off1[8] = WS_G1M + (slot0 + (lane & 7)) * 16;
  }
  wait_vm<2>();
  BARRIER();
  f32x4 acc = {0.f, 0.f, 0.f, 0.f};
  u32x4 bfo[4];
  {
    const u32 fa = (8 * g + (r >> 2)) * 128 + 32 * xw + 8 * (r & 3);
#pragma unroll
    for (int i = 0; i < 4; ++i) bfo[i] = stage_frag(lds + L_STG + fa + i * 4096);
    if (hh < FF / 64) {
#pragma unroll
      for (int i = 0; i < 2; ++i)
        acc = mfma(lds_rd128(lds + L_H + (r & 7) * H_STRIDE + 64 * i + 16 * g), stage_frag(lds + L_XN + fa + i * 4096), acc);
    }
  }
  asm volatile("" :: "v"(acc), "v"(bfo[0]), "v"(bfo[1]), "v"(bfo[2]), "v"(bfo[3]));
  poll_issue(d1, wsr, off1);
  __builtin_amdgcn_s_sleep(G1P);
  {
    u32x4 d[9];
    gather2<9>(d, d1, d2, wsr, off1, tag);
    STAMP(5);
#pragma unroll
    for (int rw = 0; rw < 2; ++rw) {
      float ms[4], ls[4];
#pragma unroll
      for (int s = 0; s < 4; ++s) {
        ms[s] = f_of(__builtin_amdgcn_readlane(d[8][0], 4 * rw + s));
        ls[s] = f_of(__builtin_amdgcn_readlane(d[8][2], 4 * rw + s));
      }
      const float M = fmaxf(fmaxf(ms[0], ms[1]), fmaxf(ms[2], ms[3]));
      float L = 0.f, o0 = 0.f, o1 = 0.f;
#pragma unroll
      for (int s = 0; s < 4; ++s) {
        const float e = __builtin_amdgcn_exp2f((ms[s] - M) * LOG2E);
        L = fmaf(e, ls[s], L); o0 = fmaf(e, f_of(d[4 * rw + s][0]), o0); o1 = fmaf(e, f_of(d[4 * rw + s][2]), o1);
      }
      const float inv = 1.0f / L;
      *(LAS u32*)(lds + L_O + (2 * xw + rw) * Q_STRIDE + lane * 4) = pack_bf(o0 * inv, o1 * inv);
    }
  }
  BARRIER();
#pragma unroll
  for (int i = 0; i < 4; ++i) acc = mfma(lds_rd128(lds + L_O + (r & 7) * Q_STRIDE + 64 * i + 16 * g), bfo[i], acc);
  if (g < 2) {
#pragma unroll
    for (int j = 0; j < 4; ++j)
      __builtin_amdgcn_raw_buffer_store_b64(u32x2{u_of(acc[j]), tag}, wsr,
                                            WS_G2 + (((4 * g + j) * 32 + 4 * nj + xw) * 32 + hh) * 128 + r * 8, 0, 16);
  }
  STAMP(10);
  if (xw != 0) { poll_retire(d1, d2); STAMP_FLUSH(); return; }
  {
    u32 off[4];
#pragma unroll
    for (int m = 0; m < 4; ++m) off[m] = WS_G2 + (fb * 32 + fcb) * 4096 + m * 1024 + lane * 16;
    u32x4 d[4], e1[4], e2[4];
    poll_issue(e1, wsr, off);
    __builtin_amdgcn_s_sleep(PSLEEP);
    gather2<4>(d, e1, e2, wsr, off, tag);
    STAMP(6);
    float v0 = (f_of(d[0][0]) + f_of(d[1][0])) + (f_of(d[2][0]) + f_of(d[3][0]));
    float v1 = (f_of(d[0][2]) + f_of(d[1][2])) + (f_of(d[2][2]) + f_of(d[3][2]));
    v0 += dpp<ROR8>(v0); v1 += dpp<ROR8>(v1);
    v0 = rows_sum(v0); v1 = rows_sum(v1);
    if (lane < 8) __hip_atomic_store((GAS u32*)(out + fb * 256 + fcb * 8 + lane), pack_bf(bf_lo(xres) + v0, bf_hi(xres) + v1),
                                     __ATOMIC_RELAXED, __HIP_MEMORY_SCOPE_AGENT);
    if (wg_id == 0 && lane == 0) __hip_atomic_store((GAS u32*)(ws + WS_EPOCH), tag, __ATOMIC_RELAXED, __HIP_MEMORY_SCOPE_AGENT);
    STAMP(7);
    poll_retire(e1, e2);
    poll_retire(d1, d2);
    STAMP_FLUSH();
  }
}

extern "C" void kernel_launch(void* const* d_in, const int* in_sizes, int n_in, void* d_out, int out_size, void* d_ws,
                              size_t ws_size, hipStream_t stream) {
  (void)in_sizes; (void)n_in; (void)out_size; (void)ws_size;
  hipLaunchKernelGGL(fused_decode_kernel, dim3(NWG), dim3(NTHR), 0, stream, (const u32*)d_in[0], (const char*)d_in[1],
                     (const char*)d_in[2], (const char*)d_in[3], (const char*)d_in[4], (const char*)d_in[6],
                     (const char*)d_in[7], (const char*)d_in[8], (u32*)d_out, (char*)d_ws);
}
```

```cpp
#include <hip/hip_runtime.h>
#include <hip/hip_bf16.h>
#include <stdint.h>
#pragma clang diagnostic ignored "-Wint-to-pointer-cast"

#define GAS __attribute__((address_space(1)))
#define LAS __attribute__((address_space(3)))
typedef unsigned u32;
typedef unsigned long long u64;
typedef u32 u32x2 __attribute__((ext_vector_type(2)));
typedef u32 u32x4 __attribute__((ext_vector_type(4)));
typedef float f32x2 __attribute__((ext_vector_type(2)));
typedef float f32x4 __attribute__((ext_vector_type(4)));
typedef __bf16 bf16x2 __attribute__((ext_vector_type(2)));
typedef __bf16 bf16x8 __attribute__((ext_vector_type(8)));
typedef short s16x4 __attribute__((ext_vector_type(4)));

#define CODE_PF 16128
#define WIN 12
#define PWIN 6
#define TAU 238
#define ASLEEP0 4
#define QSLEEP0 60
#define HSLEEP0 60
#define PSLEEP 12
#define G1P 12
#define FTRIG 8
#define DTRIG 12
#define ROTA 13
#define ROTB 8

namespace {

constexpr int DM = 512, SK = 8192, DH = 128, FF = 1792;
constexpr int NWG = 256, NTHR = 512;
constexpr int NSTEP = SK / (32 * 16);
constexpr u32 SPIN_CAP = 1u << 18;

constexpr u32 WS_EPOCH = 0;
constexpr u32 WS_G1M = 4096;
constexpr u32 WS_GQ = 32768;
constexpr u32 WS_GF = WS_GQ + 8 * 4 * 8 * 512 * 8;
constexpr u32 WS_GAP = WS_GF + 28 * 8 * 8 * 64 * 8;
constexpr u32 WS_G1 = WS_GAP + 1024;
constexpr u32 WS_G2 = WS_G1 + 8 * 4 * 8 * 4 * 1024;
constexpr u32 WS_END = WS_G2 + 8 * 32 * 32 * 128;
constexpr u32 WS_STAMP = 8u << 20;

constexpr u32 L_RING = 0;
constexpr u32 L_STG = 131072;
constexpr u32 L_XN = L_STG + 16384;
constexpr u32 XN_STRIDE = 1056;
constexpr u32 L_Q = L_XN + 8 * XN_STRIDE;
constexpr u32 Q_STRIDE = 272;
constexpr u32 L_O = L_Q + 4 * Q_STRIDE;
constexpr u32 L_H = L_O + 8 * Q_STRIDE;
constexpr u32 H_STRIDE = 144;
constexpr u32 L_CNT = L_H + 8 * H_STRIDE;
constexpr u32 L_FLAG = L_CNT + 32;
constexpr u32 L_PF = L_FLAG + 32;
constexpr u32 L_END = L_PF + 256;
constexpr u32 MRG_ML = 4 * 528;
static_assert(L_END <= 163840, "LDS");

constexpr float LOG2E = 1.4426950408889634f;
constexpr float QK_SCALE = 0.08838834764831845f;

__device__ __forceinline__ float bf_lo(u32 w) { return __builtin_bit_cast(float, w << 16); }
__device__ __forceinline__ float bf_hi(u32 w) { return __builtin_bit_cast(float, w & 0xffff0000u); }
__device__ __forceinline__ u32 pack_bf(float a, float b) {
  f32x2 v = {a, b};
  return __builtin_bit_cast(u32, __builtin_convertvector(v, bf16x2));
}
__device__ __forceinline__ float bfr(float a) { return bf_lo(pack_bf(a, a)); }
template <int CTRL> __device__ __forceinline__ float dpp(float x) {
  return __builtin_bit_cast(float, __builtin_amdgcn_mov_dpp(__builtin_bit_cast(int, x), CTRL, 0xf, 0xf, true));
}
constexpr int XOR1 = 0xB1, XOR2 = 0x4E, HMIRROR = 0x141, MIRROR = 0x140, ROR8 = 0x128;
__device__ __forceinline__ float f_of(u32 x) { return __builtin_bit_cast(float, x); }
__device__ __forceinline__ u32 u_of(float x) { return __builtin_bit_cast(u32, x); }
__device__ __forceinline__ float rows_max(float x) {
  auto s = __builtin_amdgcn_permlane16_swap(u_of(x), u_of(x), false, false);
  x = fmaxf(f_of(s[0]), f_of(s[1]));
  auto t = __builtin_amdgcn_permlane32_swap(u_of(x), u_of(x), false, false);
  return fmaxf(f_of(t[0]), f_of(t[1]));
}
__device__ __forceinline__ float rows_sum(float x) {
  auto s = __builtin_amdgcn_permlane16_swap(u_of(x), u_of(x), false, false);
  x = f_of(s[0]) + f_of(s[1]);
  auto t = __builtin_amdgcn_permlane32_swap(u_of(x), u_of(x), false, false);
  return f_of(t[0]) + f_of(t[1]);
}
__device__ __forceinline__ float wave_sum(float x) {
  x += dpp<XOR1>(x); x += dpp<XOR2>(x); x += dpp<HMIRROR>(x); x += dpp<MIRROR>(x);
  return rows_sum(x);
}

template <int W>
__device__ __forceinline__ void dma4(u32 ldsa, const void* sb, u32 v0, u32 v1, u32 v2, u32 v3) {
  u32 keep;
  asm volatile("s_mov_b32 %0, m0\n\ts_mov_b32 m0, %1\n\t"
               "s_waitcnt vmcnt(%7)\n\tglobal_load_lds_dwordx4 %3, %2 nt\n\t"
               "s_waitcnt vmcnt(%7)\n\tglobal_load_lds_dwordx4 %4, %2 offset:1024 nt\n\t"
               "s_waitcnt vmcnt(%7)\n\tglobal_load_lds_dwordx4 %5, %2 offset:2048 nt\n\t"
               "s_waitcnt vmcnt(%7)\n\tglobal_load_lds_dwordx4 %6, %2 offset:3072 nt\n\t"
               "s_mov_b32 m0, %0"
               : "=&s"(keep) : "s"(ldsa), "s"(sb), "v"(v0), "v"(v1), "v"(v2), "v"(v3), "i"(W - 1) : "memory");
}
__device__ __forceinline__ void dma1(u32 ldsa, const void* sb, u32 v0) {
  u32 keep;
  asm volatile("s_mov_b32 %0, m0\n\ts_mov_b32 m0, %1\n\ts_nop 0\n\t"
               "global_load_lds_dwordx4 %3, %2\n\t"
               "s_mov_b32 m0, %0"
               : "=&s"(keep) : "s"(ldsa), "s"(sb), "v"(v0) : "memory");
}
__device__ __forceinline__ void touch(u32 ldsa, const char* p) {
  u32 keep;
  asm volatile("s_mov_b32 %0, m0\n\ts_mov_b32 m0, %1\n\ts_nop 0\n\tglobal_load_lds_dword %2, off\n\ts_mov_b32 m0, %0"
               : "=&s"(keep) : "s"(ldsa), "v"(p) : "memory");
}
#define WAIT_LGKM0() asm volatile("s_waitcnt lgkmcnt(0)" ::: "memory")
#define BARRIER() do { asm volatile("s_waitcnt lgkmcnt(0)" ::: "memory"); __builtin_amdgcn_s_barrier(); asm volatile("" ::: "memory"); } while (0)
template <int N> __device__ __forceinline__ void wait_vm() { asm volatile("s_waitcnt vmcnt(%0)" :: "i"(N) : "memory"); }

__device__ __forceinline__ u32x4 lds_rd128(u32 a) { return *(const LAS u32x4*)a; }
__device__ __forceinline__ u32x2 lds_tr(u32 a) {
  return __builtin_bit_cast(u32x2, __builtin_amdgcn_ds_read_tr16_b64_v4i16((LAS s16x4*)a));
}
__device__ __forceinline__ f32x4 mfma(u32x4 a, u32x4 b, f32x4 c) {
  return __builtin_amdgcn_mfma_f32_16x16x32_bf16(__builtin_bit_cast(bf16x8, a), __builtin_bit_cast(bf16x8, b), c, 0, 0, 0);
}
__device__ __forceinline__ u32x4 stage_frag(u32 a) {
  const u32x2 lo = lds_tr(a), hi = lds_tr(a + 512);
  return u32x4{lo[0], lo[1], hi[0], hi[1]};
}
__device__ __forceinline__ bool gr_ok2(u32x4 v, u32 tag) { return (v[1] == tag) & (v[3] == tag); }
__device__ __forceinline__ bool all_lanes(bool ok) { return __builtin_amdgcn_ballot_w64(!ok) == 0ull; }
template <int SLEEP> __device__ __forceinline__ void wait_cnt4(u32 cnt) {
#pragma unroll 1
  for (u32 spin = 0; spin < SPIN_CAP; ++spin) {
    if (__builtin_amdgcn_readfirstlane(*(volatile LAS u32*)cnt) >= 4u) break;
    if (SLEEP) __builtin_amdgcn_s_sleep(SLEEP);
  }
  asm volatile("" ::: "memory");
}
__device__ __forceinline__ void arrive(u32 cnt, int lane) {
  WAIT_LGKM0();
  if (lane == 0) __hip_atomic_fetch_add((LAS u32*)cnt, 1u, __ATOMIC_RELAXED, __HIP_MEMORY_SCOPE_WORKGROUP);
}
__device__ __forceinline__ void aux_sync(u32 cnt, int lane) { arrive(cnt, lane); wait_cnt4<0>(cnt); }

#define STAMP(i) do { } while (0)
#define STAMP_DECL do { } while (0)
#define STAMP_FLUSH() do { } while (0)

struct StreamState {
  f32x4 acc[8];
  float m, l;
};

template <int W> __device__ __forceinline__ void issue_k(u32 ldsa, const char* src, const u32 (&vk)[4]) {
  dma4<W>(ldsa, src, vk[0], vk[1], vk[2], vk[3]);
  dma4<W>(ldsa + 4096, src + 4096, vk[0], vk[1], vk[2], vk[3]);
}
template <int W> __device__ __forceinline__ void issue_v(u32 ldsa, const char* src, const u32 (&vv)[2]) {
  dma4<W>(ldsa, src, vv[0], vv[1], vv[0], vv[1]);
  dma4<W>(ldsa + 4096, src + 4096, vv[0], vv[1], vv[0], vv[1]);
}

template <int SLOT, int WK, int WV, bool REFILL>
__device__ __forceinline__ void stream_step(StreamState& st, const u32x4 (&qf)[4], const u32 (&ka)[4], const u32 (&va)[8],
                                            u32 ring, const char* kn, const char* vn, const u32 (&vk)[4], const u32 (&vv)[2]) {
  wait_vm<WK>();
  u32x4 kf[2][4];
#pragma unroll
  for (int tt = 0; tt < 2; ++tt)
#pragma unroll
    for (int i = 0; i < 4; ++i) kf[tt][i] = lds_rd128(ka[i] + SLOT * 16384 + tt * 4096);
  WAIT_LGKM0();
  if constexpr (REFILL) issue_k<WIN>(ring + SLOT * 16384, kn, vk);
  f32x4 s0 = {0.f, 0.f, 0.f, 0.f}, s1 = {0.f, 0.f, 0.f, 0.f};
#pragma unroll
  for (int i = 0; i < 4; ++i) { s0 = mfma(kf[0][i], qf[i], s0); s1 = mfma(kf[1][i], qf[i], s1); }
  float t[8];
  {
    const u32 a = pack_bf(s0[0], s0[1]), b = pack_bf(s0[2], s0[3]), c = pack_bf(s1[0], s1[1]), d = pack_bf(s1[2], s1[3]);
    const u32 a2 = pack_bf(bf_lo(a) * QK_SCALE, bf_hi(a) * QK_SCALE), b2 = pack_bf(bf_lo(b) * QK_SCALE, bf_hi(b) * QK_SCALE);
    const u32 c2 = pack_bf(bf_lo(c) * QK_SCALE, bf_hi(c) * QK_SCALE), d2 = pack_bf(bf_lo(d) * QK_SCALE, bf_hi(d) * QK_SCALE);
    t[0] = bf_lo(a2); t[1] = bf_hi(a2); t[2] = bf_lo(b2); t[3] = bf_hi(b2);
    t[4] = bf_lo(c2); t[5] = bf_hi(c2); t[6] = bf_lo(d2); t[7] = bf_hi(d2);
  }
  float mx = fmaxf(fmaxf(fmaxf(t[0], t[1]), fmaxf(t[2], t[3])), fmaxf(fmaxf(t[4], t[5]), fmaxf(t[6], t[7])));
  mx = rows_max(mx);
  if (__builtin_amdgcn_ballot_w64(mx > st.m) != 0ull) {
    const float mn = fmaxf(st.m, mx);
    const float alpha = __builtin_amdgcn_exp2f((st.m - mn) * LOG2E);
    st.m = mn; st.l *= alpha;
#pragma unroll
    for (int c = 0; c < 8; ++c) st.acc[c] *= alpha;
  }
  const float mb = st.m * LOG2E;
  float p[8];
#pragma unroll
  for (int j = 0; j < 8; ++j) p[j] = __builtin_amdgcn_exp2f(fmaf(t[j], LOG2E, -mb));
  st.l += ((p[0] + p[1]) + (p[2] + p[3])) + ((p[4] + p[5]) + (p[6] + p[7]));
  const u32x4 pf = {pack_bf(p[0], p[1]), pack_bf(p[2], p[3]), pack_bf(p[4], p[5]), pack_bf(p[6], p[7])};
  wait_vm<WV>();
  u32x4 vf[8];
#pragma unroll
  for (int c = 0; c < 8; ++c) {
    const u32x2 lo = lds_tr(va[c] + SLOT * 16384 + 8192), hi = lds_tr(va[c] + SLOT * 16384 + 8192 + 4096);
    vf[c] = u32x4{lo[0], lo[1], hi[0], hi[1]};
  }
  WAIT_LGKM0();
  if constexpr (REFILL) issue_v<WIN>(ring + SLOT * 16384 + 8192, vn, vv);
#pragma unroll
  for (int c = 0; c < 8; ++c) st.acc[c] = mfma(vf[c], pf, st.acc[c]);
}

__device__ __forceinline__ void stage_slice(u32 stg, const char* w, u32 row_bytes, int xw, int lane) {
  const u32 vo = (lane >> 3) * row_bytes + (lane & 7) * 16;
#pragma unroll
  for (int j = 0; j < 4; ++j) dma1(stg + j * 1024, w + (size_t)(32 * xw + 8 * j) * row_bytes, vo);
}
__device__ __forceinline__ void project_slice(u32 lds, int row0, int xw, int lane, u32 cnt, __amdgpu_buffer_rsrc_t wsr, u32 dst,
                                              u32 dst_row, u32 tag) {
  const int g = lane >> 4, r = lane & 15;
  const u32 stg = lds + L_STG + xw * 4096;
  const u32x4 a = lds_rd128(lds + L_XN + (r & 7) * XN_STRIDE + (row0 + 32 * xw + 8 * g) * 2);
  const u32 ba = stg + (8 * g + (r >> 2)) * 128 + 8 * (r & 3);
  u32x4 bq[4];
#pragma unroll
  for (int j = 0; j < 4; ++j) bq[j] = stage_frag(ba + 32 * j);
  f32x4 c[4];
#pragma unroll
  for (int j = 0; j < 4; ++j) c[j] = mfma(a, bq[j], f32x4{0.f, 0.f, 0.f, 0.f});
  WAIT_LGKM0();
  if (g < 2) {
#pragma unroll
    for (int j = 0; j < 4; ++j) *(LAS f32x4*)(stg + j * 512 + r * 32 + g * 16) = c[j];
  }
  aux_sync(cnt, lane);
  const int ob = (xw * 64 + lane) >> 5, op = lane & 31;
  const u32 ra = lds + L_STG + (op >> 3) * 512 + ((2 * op) & 15) * 32 + ob * 4;
  float x0[4], x1[4];
#pragma unroll
  for (int w = 0; w < 4; ++w) { x0[w] = *(const LAS float*)(ra + w * 4096); x1[w] = *(const LAS float*)(ra + w * 4096 + 32); }
  const float v0 = (x0[0] + x0[1]) + (x0[2] + x0[3]), v1 = (x1[0] + x1[1]) + (x1[2] + x1[3]);
  __builtin_amdgcn_raw_buffer_store_b128(u32x4{u_of(v0), tag, u_of(v1), tag}, wsr, dst + ob * dst_row + op * 16, 0, 16);
}
template <int N>
__device__ __forceinline__ void poll_issue(u32x4 (&a)[N], __amdgpu_buffer_rsrc_t wsr, const u32 (&off)[N]) {
  asm volatile("" ::: "memory");
#pragma unroll
  for (int k = 0; k < N; ++k) a[k] = __builtin_amdgcn_raw_buffer_load_b128(wsr, off[k], 0, 16);
}
template <int N>
__device__ __forceinline__ bool poll_ok(u32x4 (&a)[N], u32 tag) {
#pragma unroll
  for (int k = 0; k < N; ++k) asm volatile("" : "+v"(a[k]) :: "memory");
  bool ok = true;
#pragma unroll
  for (int k = 0; k < N; ++k) ok = ok & gr_ok2(a[k], tag);
  return all_lanes(ok);
}
template <int N>
__device__ __forceinline__ void gather2(u32x4 (&r)[N], u32x4 (&a)[N], u32x4 (&b)[N], __amdgpu_buffer_rsrc_t wsr, const u32 (&off)[N],
                                        u32 tag) {
#pragma unroll 1
  for (u32 spin = 0; spin < SPIN_CAP; ++spin) {
    poll_issue(b, wsr, off);
    if (poll_ok(a, tag)) {
#pragma unroll
      for (int k = 0; k < N; ++k) r[k] = a[k];
      return;
    }
    poll_issue(a, wsr, off);
    if (poll_ok(b, tag)) {
#pragma unroll
      for (int k = 0; k < N; ++k) r[k] = b[k];
      return;
    }
  }
#pragma unroll
  for (int k = 0; k < N; ++k) r[k] = b[k];
}
template <int N> __device__ __forceinline__ void poll_retire(u32x4 (&a)[N], u32x4 (&b)[N]) {
#pragma unroll
  for (int k = 0; k < N; ++k) asm volatile("" :: "v"(a[k]), "v"(b[k]));
}
template <int N, int SLEEP>
__device__ __forceinline__ void gather1(u32x4 (&v)[N], __amdgpu_buffer_rsrc_t wsr, u32 off, u32 stride, u32 tag) {
#pragma unroll 1
  for (u32 spin = 0; spin < SPIN_CAP; ++spin) {
    asm volatile("" ::: "memory");
#pragma unroll
    for (int k = 0; k < N; ++k) v[k] = __builtin_amdgcn_raw_buffer_load_b128(wsr, off + k * stride, 0, 16);
    bool ok = true;
#pragma unroll
    for (int k = 0; k < N; ++k) ok = ok & gr_ok2(v[k], tag);
    if (all_lanes(ok)) break;
    if (SLEEP) __builtin_amdgcn_s_sleep(SLEEP);
  }
}

}

__global__ void __launch_bounds__(NTHR, 2)
fused_decode_kernel(const u32* __restrict__ x_, const char* __restrict__ kc, const char* __restrict__ vc, const char* __restrict__ wq,
                    const char* __restrict__ wo, const char* __restrict__ wg, const char* __restrict__ wu, const char* __restrict__ wd,
                    u32* __restrict__ out, char* __restrict__ ws) {
  __shared__ __attribute__((aligned(1024))) char smem[L_END];
  u64 pc;
  asm volatile("s_getpc_b64 %0" : "=s"(pc));
  const u32 lds = (u32)(size_t)(LAS char*)smem;
  const int tid = threadIdx.x, lane = tid & 63;
  const int wave = __builtin_amdgcn_readfirstlane(tid >> 6);
  if constexpr (CODE_PF > 0) {
    constexpr int NPF = (CODE_PF + 8191) / 8192;
    if (wave < NPF) {
      u32 keep;
      const u64 base = (pc & ~0xFFull) + (u64)(wave < NPF - 1 ? wave * 8192 : CODE_PF - 8192);
      asm volatile("s_mov_b32 %0, m0\n\ts_mov_b32 m0, %1\n\ts_nop 0\n\tglobal_load_lds_dword %3, %2\n\ts_mov_b32 m0, %0"
                   : "=&s"(keep) : "s"(lds + L_PF), "s"(base), "v"(lane * 128) : "memory");
    }
  }
  const int wg_id = blockIdx.x;
  const int xg = wg_id & 7, rr = wg_id >> 3;
  const int b = xg, kvh = rr >> 2, sp = rr & 3;
  const int g = lane >> 4, r = lane & 15;
  STAMP_DECL;
  STAMP(0);

  if (wave < 4) {
    if (wave == 3 && rr == 31) {
      const char* p = lane < 5    ? wo + (size_t)xg * 524288 + min(lane * 131072, 524284)
                      : lane < 8  ? wd + min(xg * 262144 + (lane - 5) * 131072, 1835004)
                      : lane < 22 ? wg + (lane - 8) * 131072
                      : lane < 36 ? wu + (lane - 22) * 131072
                      : lane == 36 ? ws + WS_GAP : lane == 37 ? ws + WS_END
                      : lane == 38 ? (const char*)out
                                   : (lane < 47 ? vc : kc) + (size_t)(b * 8 + ((lane - 39) & 7)) * (SK * DH * 2) + 1048576;
      if (lane < 55) touch(lds + L_PF, p);
    }
    const size_t blk = (size_t)(b * 8 + kvh) * SK * (DH * 2);
    const char* kb = kc + blk;
    const char* vb = vc + blk;
    const u32 ring = lds + L_RING + wave * 32768;
    const int rot = (kvh * ROTA + b * ROTB) & (NSTEP - 1), v16 = sp * 4 + wave;
    auto tile_off = [&](int s) -> u32 { const int w = (s + rot) & (NSTEP - 1); return w * 131072 + ((v16 ^ (w & 1)) << 13); };
    u32 vk[4], vv[2];
#pragma unroll
    for (int n = 0; n < 4; ++n) vk[n] = g * 256 + (((r ^ g) ^ (4 * n)) << 4);
#pragma unroll
    for (int n = 0; n < 2; ++n) vv[n] = g * 256 + ((r ^ ((4 * n + g) << 1)) << 4);
    if constexpr (ASLEEP0 > 0) {
      asm volatile("" :: "s"(kb), "s"(vb));
      __builtin_amdgcn_s_sleep(ASLEEP0);
    }
    const u32 tstart = (u32)__builtin_amdgcn_s_memrealtime();
    {
      const u32 o0 = tile_off(0), o1 = tile_off(1);
      issue_k<PWIN>(ring, kb + o0, vk); issue_v<PWIN>(ring + 8192, vb + o0, vv);
      issue_k<PWIN>(ring + 16384, kb + o1, vk); issue_v<PWIN>(ring + 16384 + 8192, vb + o1, vv);
    }
    STAMP(1);
    BARRIER();
    STAMP(2);
    u32x4 qf[4];
#pragma unroll
    for (int i = 0; i < 4; ++i) {
      const u32x4 v = lds_rd128(lds + L_Q + (r & 3) * Q_STRIDE + 64 * i + 16 * g);
      qf[i] = r < 4 ? v : u32x4{0u, 0u, 0u, 0u};
    }
    u32 ka[4], va[8];
#pragma unroll
    for (int i = 0; i < 4; ++i) ka[i] = ring + r * 256 + ((((4 * i + g) ^ r)) << 4);
    {
      const int q4 = r >> 2, p4 = r & 3, rho = (4 * g + q4) & 7;
      const u32 base = (4 * g + q4) * 256 + 16 * (p4 >> 1) + 8 * (p4 & 1);
#pragma unroll
      for (int c = 0; c < 8; ++c) va[c] = ring + base + ((c ^ rho) << 5);
    }
    StreamState st;
#pragma unroll
    for (int c = 0; c < 8; ++c) st.acc[c] = f32x4{0.f, 0.f, 0.f, 0.f};
    st.m = -INFINITY; st.l = 0.f;
    const u32 tnow = (u32)__builtin_amdgcn_s_memrealtime();
    const int left = (int)(tstart + (NSTEP - 1) * TAU - tnow);
    const u32 tau = left > 0 ? ((u32)left * 5041u) >> 16 : 0u, torg = tnow - 2 * tau;
    auto pace = [&](int s) {
      if constexpr (TAU > 0) { while ((int)((u32)__builtin_amdgcn_s_memrealtime() - (torg + s * tau)) < 0) __builtin_amdgcn_s_sleep(2); }
    };
#pragma unroll 1
    for (int t = 0; t < NSTEP - 2; t += 2) {
      const u32 o0 = tile_off(t + 2), o1 = tile_off(t + 3);
      pace(t + 2);
      stream_step<0, 24, 24, true>(st, qf, ka, va, ring, kb + o0, vb + o0, vk, vv);
      pace(t + 3);
      stream_step<1, 24, 24, true>(st, qf, ka, va, ring, kb + o1, vb + o1, vk, vv);
      if (t == FTRIG) arrive(lds + L_CNT + 16, lane);
      if (t == DTRIG) arrive(lds + L_CNT + 20, lane);
    }
    stream_step<0, 24, 16, false>(st, qf, ka, va, ring, kb, vb, vk, vv);
    stream_step<1, 8, 0, false>(st, qf, ka, va, ring, kb, vb, vk, vv);
    STAMP(3);
    const float lsum = rows_sum(st.l);
    if (r < 4) {
#pragma unroll
      for (int c = 0; c < 8; ++c) *(LAS f32x4*)(ring + r * 528 + (16 * c + 4 * g) * 4) = st.acc[c];
      if (g == 0) *(LAS f32x2*)(ring + MRG_ML + r * 8) = f32x2{st.m, lsum};
    }
    BARRIER();
    STAMP(4);
    STAMP_FLUSH();
    return;
  }

  __builtin_amdgcn_s_setprio(3);
  const int xw = wave - 4;
  const u32 stg = lds + L_STG + xw * 4096;
  const u32x4 xa = ((const GAS u32x4*)x_)[(2 * xw) * 64 + lane];
  const u32x4 xb = ((const GAS u32x4*)x_)[(2 * xw + 1) * 64 + lane];
  const int qk = rr >> 3, qc = rr & 7;
  stage_slice(stg, wq + (size_t)(128 * qk) * 8192 + (xg * 512 + qc * 64) * 2, 8192, xw, lane);
  const u32 epoch = __hip_atomic_load((GAS u32*)(ws + WS_EPOCH), __ATOMIC_RELAXED, __HIP_MEMORY_SCOPE_AGENT);
  if (tid < 256 + 8) *(LAS u32*)(lds + L_CNT + (tid - 256) * 4) = 0u;
  const __amdgpu_buffer_rsrc_t wsr = __builtin_amdgcn_make_buffer_rsrc(ws, 0, WS_END, 0x00020000);
#pragma unroll
  for (int rw = 0; rw < 2; ++rw) {
    const u32x4 xv = rw ? xb : xa;
    float v[8] = {bf_lo(xv[0]), bf_hi(xv[0]), bf_lo(xv[1]), bf_hi(xv[1]), bf_lo(xv[2]), bf_hi(xv[2]), bf_lo(xv[3]), bf_hi(xv[3])};
    const float mean = wave_sum(((v[0] + v[1]) + (v[2] + v[3])) + ((v[4] + v[5]) + (v[6] + v[7]))) * (1.0f / DM);
    float ss = 0.f;
#pragma unroll
    for (int j = 0; j < 8; ++j) { const float d = v[j] - mean; ss = fmaf(d, d, ss); }
    const float var = wave_sum(ss) * (1.0f / DM);
    const float mb = bfr(mean), den = bfr(sqrtf(bfr(bfr(var) + 1e-5f)));
    const float rd = 1.0f / den;
    float y[8];
#pragma unroll
    for (int j = 0; j < 8; ++j) {
      const float d = bfr(v[j] - mb), q0 = d * rd;
      y[j] = fmaf(fmaf(-q0, den, d), rd, q0);
    }
    *(LAS u32x4*)(lds + L_XN + (2 * xw + rw) * XN_STRIDE + lane * 16) =
        u32x4{pack_bf(y[0], y[1]), pack_bf(y[2], y[3]), pack_bf(y[4], y[5]), pack_bf(y[6], y[7])};
  }
  STAMP(9);
  const u32 tag = epoch + 1u;
  wait_vm<0>();
  {
    WAIT_LGKM0();
    if (lane == 0) *(volatile LAS u32x2*)(lds + L_FLAG + xw * 8) = u32x2{tag, ~tag};
#pragma unroll 1
    for (u32 spin = 0; spin < SPIN_CAP; ++spin) {
      const u32x4 f0 = *(volatile LAS u32x4*)(lds + L_FLAG), f1 = *(volatile LAS u32x4*)(lds + L_FLAG + 16);
      const bool ok = (f0[0] == tag) & (f0[1] == ~tag) & (f0[2] == tag) & (f0[3] == ~tag) & (f1[0] == tag) & (f1[1] == ~tag) &
                      (f1[2] == tag) & (f1[3] == ~tag);
      if (__builtin_amdgcn_readfirstlane(ok)) break;
    }
    asm volatile("" ::: "memory");
  }
  project_slice(lds, 128 * qk, xw, lane, lds + L_CNT + 0, wsr, WS_GQ + ((xg * 4 + qk) * 8 * 512 + qc * 64) * 8, 512 * 8, tag);
  STAMP(11);
  {
    u32x4 v[4];
    __builtin_amdgcn_s_sleep(QSLEEP0);
    gather1<4, 0>(v, wsr, WS_GQ + ((kvh * 4 * 8 + b) * 512 + xw * 128 + 2 * lane) * 8, 8 * 512 * 8, tag);
    const float q0 = (f_of(v[0][0]) + f_of(v[1][0])) + (f_of(v[2][0]) + f_of(v[3][0]));
    const float q1 = (f_of(v[0][2]) + f_of(v[1][2])) + (f_of(v[2][2]) + f_of(v[3][2]));
    *(LAS u32*)(lds + L_Q + xw * Q_STRIDE + lane * 4) = pack_bf(q0, q1);
  }
  STAMP(2);
  BARRIER();

  const int hh = xg * 4 + (rr >> 3), nj = rr & 7;
  const int fb = rr >> 2, fcb = xg * 4 + (rr & 3);
  const u32 xres = ((const GAS u32*)x_)[fb * 256 + fcb * 8 + (lane & 7)];
  const char* wo_src = wo + (size_t)(hh * 128) * 1024 + nj * 128;
  const char* wd_src = wd + (size_t)(min(hh, FF / 64 - 1) * 64) * 1024 + nj * 128;
  if (xw == 0 && lane < 5) touch(lds + L_PF, lane == 0 ? (const char*)out + fb * 1024 : lane == 1 ? ws + WS_GAP : lane == 2 ? ws + WS_END
                                              : lane == 3 ? wo_src : wd_src);
  wait_cnt4<16>(lds + L_CNT + 16);
  if (rr < 28) {
    const int ta = xg * 28 + rr, fm = ta & 1, fk = (ta >> 1) & 3, fc = ta >> 3;
    stage_slice(stg, (fm ? wu : wg) + (size_t)(128 * fk) * (FF * 2) + fc * 128, FF * 2, xw, lane);
    wait_vm<0>();
    project_slice(lds, 128 * fk, xw, lane, lds + L_CNT + 4, wsr, WS_GF + (((fc * 2 + fm) * 4 + fk) * 8 * 64) * 8, 64 * 8, tag);
  }
  STAMP(12);
  aux_sync(lds + L_CNT + 8, lane);
  if (hh < FF / 64) {
    const int hb = (xw * 64 + lane) >> 5, hp = lane & 31;
    const u32 ha = WS_GF + ((hh * 8 * 8 + hb) * 64 + 2 * hp) * 8;
    float gu[2][2];
    __builtin_amdgcn_s_sleep(HSLEEP0);
#pragma unroll
    for (int m = 0; m < 2; ++m) {
      u32x4 v[4];
      gather1<4, 32>(v, wsr, ha + m * 4 * (8 * 64 * 8), 8 * 64 * 8, tag);
      gu[m][0] = (f_of(v[0][0]) + f_of(v[1][0])) + (f_of(v[2][0]) + f_of(v[3][0]));
      gu[m][1] = (f_of(v[0][2]) + f_of(v[1][2])) + (f_of(v[2][2]) + f_of(v[3][2]));
    }
    *(LAS u32*)(lds + L_H + hb * H_STRIDE + hp * 4) = pack_bf(bfr(gu[0][0]) * bfr(gu[1][0]), bfr(gu[0][1]) * bfr(gu[1][1]));
  }
  STAMP(13);
  wait_cnt4<16>(lds + L_CNT + 20);
  stage_slice(stg, wo_src, 1024, xw, lane);
  if (hh < FF / 64) {
    const u32 vo = (lane >> 3) * 1024 + (lane & 7) * 16;
#pragma unroll
    for (int j = 0; j < 2; ++j) dma1(lds + L_XN + (16 * xw + 8 * j) * 128, wd_src + (16 * xw + 8 * j) * 1024, vo);
  }
  STAMP(14);
  BARRIER();
  STAMP(4);
  {
    float mw[4], lw[4]; f32x2 ow[4];
#pragma unroll
    for (int w = 0; w < 4; ++w) {
      const f32x2 ml = *(const LAS f32x2*)(lds + L_RING + w * 32768 + MRG_ML + xw * 8);
      mw[w] = ml[0]; lw[w] = ml[1];
      ow[w] = *(const LAS f32x2*)(lds + L_RING + w * 32768 + xw * 528 + lane * 8);
    }
    const float M = fmaxf(fmaxf(mw[0], mw[1]), fmaxf(mw[2], mw[3]));
    float L = 0.f, o0 = 0.f, o1 = 0.f;
#pragma unroll
    for (int w = 0; w < 4; ++w) {
      const float e = __builtin_amdgcn_exp2f((mw[w] - M) * LOG2E);
      L = fmaf(e, lw[w], L); o0 = fmaf(e, ow[w][0], o0); o1 = fmaf(e, ow[w][1], o1);
    }
    const u32 slot = ((kvh * 4 + xw) * 8 + b) * 4 + sp;
    __builtin_amdgcn_raw_buffer_store_b128(u32x4{u_of(o0), tag, u_of(o1), tag}, wsr, WS_G1 + slot * 1024 + lane * 16, 0, 16);
    if (lane == 0) __builtin_amdgcn_raw_buffer_store_b128(u32x4{u_of(M), tag, u_of(L), tag}, wsr, WS_G1M + slot * 16, 0, 16);
  }
  STAMP(8);
  u32 off1[9];
  u32x4 d1[9], d2[9];
  {
    const u32 slot0 = (hh * 8 + 2 * xw) * 4;
#pragma unroll
    for (int s = 0; s < 8; ++s) off1[s] = WS_G1 + (slot0 + s) * 1024 + lane * 16;
    off1[8] = WS_G1M + (slot0 + (lane & 7)) * 16;
  }
  wait_vm<2>();
  BARRIER();
  f32x4 acc = {0.f, 0.f, 0.f, 0.f};
  u32x4 bfo[4];
  {
    const u32 fa = (8 * g + (r >> 2)) * 128 + 32 * xw + 8 * (r & 3);
#pragma unroll
    for (int i = 0; i < 4; ++i) bfo[i] = stage_frag(lds + L_STG + fa + i * 4096);
    if (hh < FF / 64) {
#pragma unroll
      for (int i = 0; i < 2; ++i)
        acc = mfma(lds_rd128(lds + L_H + (r & 7) * H_STRIDE + 64 * i + 16 * g), stage_frag(lds + L_XN + fa + i * 4096), acc);
    }
  }
  asm volatile("" :: "v"(acc), "v"(bfo[0]), "v"(bfo[1]), "v"(bfo[2]), "v"(bfo[3]));
  poll_issue(d1, wsr, off1);
  __builtin_amdgcn_s_sleep(G1P);
  {
    u32x4 d[9];
    gather2<9>(d, d1, d2, wsr, off1, tag);
    STAMP(5);
#pragma unroll
    for (int rw = 0; rw < 2; ++rw) {
      float ms[4], ls[4];
#pragma unroll
      for (int s = 0; s < 4; ++s) {
        ms[s] = f_of(__builtin_amdgcn_readlane(d[8][0], 4 * rw + s));
        ls[s] = f_of(__builtin_amdgcn_readlane(d[8][2], 4 * rw + s));
      }
      const float M = fmaxf(fmaxf(ms[0], ms[1]), fmaxf(ms[2], ms[3]));
      float L = 0.f, o0 = 0.f, o1 = 0.f;
#pragma unroll
      for (int s = 0; s < 4; ++s) {
        const float e = __builtin_amdgcn_exp2f((ms[s] - M) * LOG2E);
        L = fmaf(e, ls[s], L); o0 = fmaf(e, f_of(d[4 * rw + s][0]), o0); o1 = fmaf(e, f_of(d[4 * rw + s][2]), o1);
      }
      const float inv = 1.0f / L;
      *(LAS u32*)(lds + L_O + (2 * xw + rw) * Q_STRIDE + lane * 4) = pack_bf(o0 * inv, o1 * inv);
    }
  }
  BARRIER();
#pragma unroll
  for (int i = 0; i < 4; ++i) acc = mfma(lds_rd128(lds + L_O + (r & 7) * Q_STRIDE + 64 * i + 16 * g), bfo[i], acc);
  if (g < 2) {
#pragma unroll
    for (int j = 0; j < 4; ++j)
      __builtin_amdgcn_raw_buffer_store_b64(u32x2{u_of(acc[j]), tag}, wsr,
                                            WS_G2 + (((4 * g + j) * 32 + 4 * nj + xw) * 32 + hh) * 128 + r * 8, 0, 16);
  }
  STAMP(10);
  if (xw != 0) { poll_retire(d1, d2); STAMP_FLUSH(); return; }
  {
    u32 off[4];
#pragma unroll
    for (int m = 0; m < 4; ++m) off[m] = WS_G2 + (fb * 32 + fcb) * 4096 + m * 1024 + lane * 16;
    u32x4 d[4], e1[4], e2[4];
    poll_issue(e1, wsr, off);
    __builtin_amdgcn_s_sleep(PSLEEP);
    gather2<4>(d, e1, e2, wsr, off, tag);
    STAMP(6);
    float v0 = (f_of(d[0][0]) + f_of(d[1][0])) + (f_of(d[2][0]) + f_of(d[3][0]));
    float v1 = (f_of(d[0][2]) + f_of(d[1][2])) + (f_of(d[2][2]) + f_of(d[3][2]));
    v0 += dpp<ROR8>(v0); v1 += dpp<ROR8>(v1);
    v0 = rows_sum(v0); v1 = rows_sum(v1);
    if (lane < 8) __hip_atomic_store((GAS u32*)(out + fb * 256 + fcb * 8 + lane), pack_bf(bf_lo(xres) + v0, bf_hi(xres) + v1),
                                     __ATOMIC_RELAXED, __HIP_MEMORY_SCOPE_AGENT);
    if (wg_id == 0 && lane == 0) __hip_atomic_store((GAS u32*)(ws + WS_EPOCH), tag, __ATOMIC_RELAXED, __HIP_MEMORY_SCOPE_AGENT);
    STAMP(7);
    poll_retire(e1, e2);
    poll_retire(d1, d2);
    STAMP_FLUSH();
  }
}

extern "C" void kernel_launch(void* const* d_in, const int* in_sizes, int n_in, void* d_out, int out_size, void* d_ws,
                              size_t ws_size, hipStream_t stream) {
  (void)in_sizes; (void)n_in; (void)out_size; (void)ws_size;
  hipLaunchKernelGGL(fused_decode_kernel, dim3(NWG), dim3(NTHR), 0, stream, (const u32*)d_in[0], (const char*)d_in[1],
                     (const char*)d_in[2], (const char*)d_in[3], (const char*)d_in[4], (const char*)d_in[6],
                     (const char*)d_in[7], (const char*)d_in[8], (u32*)d_out, (char*)d_ws);
}
```
